```python
import torch

def _softmax(x, axis=-1):
    return torch.softmax(x, dim=axis)
def _gelu(x):
    return torch.nn.functional.gelu(x, approximate="tanh")
def _silu(x):
    return torch.nn.functional.silu(x)
def _sigmoid(x):
    return torch.sigmoid(x)
def _layernorm(x, axis=-1, eps=1e-5):
    m = x.mean(dim=axis, keepdim=True)
    v = x.var(dim=axis, keepdim=True, unbiased=False)
    return (x - m) / torch.sqrt(v + eps)
def _scatter_add(tgt, idx, src, axis=0):
    return tgt.clone().index_add_(axis, idx, src)
def _segment_sum(src, seg, n):
    out = torch.zeros((n,) + src.shape[1:], dtype=src.dtype)
    return out.index_add_(0, seg, src)
def _pad(x, pw):
    flat = tuple(v for pair in reversed(pw) for v in pair)
    return torch.nn.functional.pad(x, flat)

kernel_name = 'standalone__gqa_block_cached__near_ridge'

def setup_inputs():

    _w = (lambda a: (lambda t: t.to(torch.bfloat16) if t.is_floating_point() else t)(torch.as_tensor(a)))
    B, Sq, D = 8, 112, 448
    Hq, Hkv, Sk, Dh = 16, 8, 1024, 256
    assert Hq % Hkv == 0, (Hq, Hkv)
    return {
        "x": _w((lambda *s: torch.randn(*s, dtype=torch.bfloat16) * 0.8)(B, Sq, D)),
        "k_cache": _w((lambda *s: torch.randn(*s, dtype=torch.bfloat16) * 0.8)(B, Hkv, Sk, Dh)),
        "v_cache": _w((lambda *s: torch.randn(*s, dtype=torch.bfloat16) * 0.05)(B, Hkv, Sk, Dh)),
        "wq": _w((lambda *s: torch.randn(*s, dtype=torch.bfloat16) * 0.8)(D, Hq * Dh) * (D ** -0.5)),
        "wo": _w((lambda *s: torch.randn(*s, dtype=torch.bfloat16) * 0.05)(Hq * Dh, D) * ((Hq * Dh) ** -0.5)),
    }

def reference(x, k_cache, v_cache, wq, wo):
    B, Sq, D = x.shape
    Hq, Dh = 16, 256
    Hkv = k_cache.shape[1]
    rep = Hq // Hkv
    q = (lambda x, *d: x.permute(*d))((x @ wq).reshape(B, Sq, Hq, Dh), 0, 2, 1, 3)
    k = (lambda x, n, axis: torch.repeat_interleave(x, n, dim=axis))(k_cache, rep, axis=1)
    v = (lambda x, n, axis: torch.repeat_interleave(x, n, dim=axis))(v_cache, rep, axis=1)
    scores = torch.einsum("bhqd,bhkd->bhqk", q, k) * (Dh ** -0.5)
    p = _softmax(scores, axis=-1)
    o = torch.einsum("bhqk,bhkd->bhqd", p, v)
    return (lambda x, *d: x.permute(*d))(o, 0, 2, 1, 3).reshape(B, Sq, Hq * Dh) @ wo
```

```cpp
#include <hip/hip_runtime.h>
#include <hip/hip_bf16.h>
#include <stdint.h>

typedef short bf16x8 __attribute__((ext_vector_type(8)));
typedef short s16x4 __attribute__((ext_vector_type(4)));
typedef float f32x16 __attribute__((ext_vector_type(16)));
typedef unsigned u32x4 __attribute__((ext_vector_type(4)));
typedef unsigned u32x2 __attribute__((ext_vector_type(2)));
typedef __attribute__((address_space(3))) s16x4* lds_s16x4_ptr;

constexpr int NB = 8, SQ = 112, DM = 448, HQ = 16, HKV = 8, SK = 1024, DH = 256;
constexpr int QR = 28;

#define MFMA32(a, b, c) __builtin_amdgcn_mfma_f32_32x32x16_bf16((a), (b), (c), 0, 0, 0)
#define DI __device__ __forceinline__

DI unsigned cvtpk(float lo, float hi) {
  unsigned r;
  asm volatile("v_cvt_pk_bf16_f32 %0, %1, %2" : "=v"(r) : "v"(lo), "v"(hi));
  return r;
}
DI s16x4 tr_read(const char* p) {
  return __builtin_amdgcn_ds_read_tr16_b64_v4i16((lds_s16x4_ptr)(p));
}
DI void glds16(const void* g, char* l) {
  __builtin_amdgcn_global_load_lds((const unsigned*)g, (unsigned*)l, 16, 0, 0);
}
DI bf16x8 cat8(s16x4 lo, s16x4 hi) { return __builtin_shufflevector(lo, hi, 0, 1, 2, 3, 4, 5, 6, 7); }

#define WAIT_VM0() asm volatile("s_waitcnt vmcnt(0)" ::: "memory")
#define WAIT_LGKM0() asm volatile("s_waitcnt lgkmcnt(0)" ::: "memory")
#define BARRIER() do { asm volatile("" ::: "memory"); __builtin_amdgcn_s_barrier(); asm volatile("" ::: "memory"); } while (0)

constexpr int LDS_K = 0;
constexpr int LDS_V = 65536;
constexpr int LDS_Q = 131072;
constexpr int LDS_TOTAL = 163840;

__global__ __launch_bounds__(256, 1)
void attn_kernel(const __hip_bfloat16* __restrict__ x, const __hip_bfloat16* __restrict__ kc,
                 const __hip_bfloat16* __restrict__ vc, const __hip_bfloat16* __restrict__ wq,
                 __hip_bfloat16* __restrict__ o_ws) {
  __shared__ __attribute__((aligned(16))) char smem[LDS_TOTAL];
  const int tid = threadIdx.x, w = tid >> 6, lane = tid & 63, r32 = lane & 31, hi = lane >> 5;
  const int i16 = lane & 15, q4 = i16 >> 2, p4 = i16 & 3, g = lane >> 4, hh = g >> 1, cb = g & 1;
  const int bid = blockIdx.x;
  const int b = bid & 7, rest = bid >> 3, kvh = rest >> 2, quarter = rest & 3;
  const int qt = w & 1, kh = w >> 1;

  f32x16 qacc[2][2];
#pragma unroll
  for (int a = 0; a < 2; ++a)
#pragma unroll
    for (int t = 0; t < 2; ++t)
#pragma unroll
      for (int r = 0; r < 16; ++r) qacc[a][t][r] = 0.f;
  {
    const int xr = quarter * QR + (r32 < QR ? r32 : QR - 1);
    const __hip_bfloat16* xp = x + ((long)b * SQ + xr) * DM + 8 * hi;
    bf16x8 xf[28];
#pragma unroll
    for (int s = 0; s < 28; ++s) xf[s] = *reinterpret_cast<const bf16x8*>(xp + 16 * s);

    char* stg = smem + w * 32768;
    const int srow = lane >> 4;
    const int sc = (lane & 15) ^ (srow << 2);
    const __hip_bfloat16* wsrc = wq + (long)srow * (HQ * DH) + (kvh * 2 + (sc >> 3)) * DH + 64 * w + 8 * (sc & 7);
    auto stage = [&](int c) {
      char* dst = stg + (c & 1) * 16384;
#pragma unroll
      for (int p = 0; p < 16; ++p)
        glds16(wsrc + (long)(64 * c + 4 * p) * (HQ * DH), dst + p * 1024);
    };
    stage(0);
    stage(1);
    int tra[4];
#pragma unroll
    for (int ha = 0; ha < 4; ++ha)
      tra[ha] = (8 * hh + q4) * 256 + 16 * (((4 * ha + 2 * cb + (p4 >> 1)) ^ (q4 << 2))) + 8 * (p4 & 1);
#pragma unroll
    for (int c = 0; c < 7; ++c) {
      if (c < 6) asm volatile("s_waitcnt vmcnt(16)" ::: "memory");
      else WAIT_VM0();
      const char* src = stg + (c & 1) * 16384;
#pragma unroll
      for (int s = 0; s < 4; ++s) {
#pragma unroll
        for (int ha = 0; ha < 4; ++ha) {
          s16x4 lo = tr_read(src + tra[ha] + s * 4096);
          s16x4 hi4 = tr_read(src + tra[ha] + s * 4096 + 1024);
          qacc[ha & 1][ha >> 1] = MFMA32(cat8(lo, hi4), xf[4 * c + s], qacc[ha & 1][ha >> 1]);
        }
      }
      if (c + 2 < 7) {
        WAIT_LGKM0();
        stage(c + 2);
      }
    }
  }
#pragma unroll
  for (int a = 0; a < 2; ++a)
#pragma unroll
    for (int t = 0; t < 2; ++t)
#pragma unroll
      for (int gq = 0; gq < 4; ++gq) {
        u32x2 v;
        v[0] = cvtpk(qacc[a][t][4 * gq + 0], qacc[a][t][4 * gq + 1]);
        v[1] = cvtpk(qacc[a][t][4 * gq + 2], qacc[a][t][4 * gq + 3]);
        const int row = 32 * t + r32, ch = 8 * w + 4 * a + gq;
        *reinterpret_cast<u32x2*>(smem + LDS_Q + row * 512 + 16 * (ch ^ (row & 15)) + 8 * hi) = v;
      }
  WAIT_LGKM0();
  BARRIER();

  const long kvbase = ((long)(b * HKV + kvh) * SK + kh * 512) * DH;
  int koff[8], voff[8];
#pragma unroll
  for (int p = 0; p < 8; ++p) {
    const int row = 16 * qt + 2 * p + hi;
    koff[p] = row * DH + 8 * (r32 ^ (row & 15));
    voff[p] = row * DH + 8 * (r32 ^ ((row & 3) << 2));
  }
  const __hip_bfloat16* kg = kc + kvbase;
  const __hip_bfloat16* vg = vc + kvbase;
  char* kdst = smem + LDS_K + kh * 16384 + qt * 8192;
  char* vdst = smem + LDS_V + kh * 16384 + qt * 8192;
  auto stageK = [&](int j) {
#pragma unroll
    for (int p = 0; p < 8; ++p) glds16(kg + (long)j * 32 * DH + koff[p], kdst + (j & 1) * 32768 + p * 1024);
  };
  auto stageV = [&](int j) {
#pragma unroll
    for (int p = 0; p < 8; ++p) glds16(vg + (long)j * 32 * DH + voff[p], vdst + (j & 1) * 32768 + p * 1024);
  };
  stageK(0);
  stageV(0);
  stageK(1);

  bf16x8 qf[16];
  {
    const int row = 32 * qt + r32;
#pragma unroll
    for (int s = 0; s < 16; ++s)
      qf[s] = *reinterpret_cast<const bf16x8*>(smem + LDS_Q + row * 512 + 16 * ((2 * s + hi) ^ (row & 15)));
  }

  int kra[8];
#pragma unroll
  for (int s = 0; s < 8; ++s) kra[s] = LDS_K + kh * 16384 + r32 * 512 + 16 * ((2 * s + hi) ^ (r32 & 15));
  int vra[4];
#pragma unroll
  for (int t = 0; t < 4; ++t)
    vra[t] = LDS_V + kh * 16384 + (4 * hh + q4) * 512 + 16 * (4 * (t ^ q4) + 2 * cb + (p4 >> 1)) + 8 * (p4 & 1);

  f32x16 o[8];
#pragma unroll
  for (int t = 0; t < 8; ++t)
#pragma unroll
    for (int r = 0; r < 16; ++r) o[t][r] = 0.f;
  float m_run = -1e30f, l_run = 0.f;
  constexpr float SC = 0.0625f * 1.4426950408889634f;
  constexpr float THR = 8.0f / 0.0625f;

  auto qk = [&](int buf) {
    f32x16 s_;
#pragma unroll
    for (int r = 0; r < 16; ++r) s_[r] = 0.f;
#pragma unroll
    for (int s = 0; s < 16; ++s) {
      bf16x8 kf = *reinterpret_cast<const bf16x8*>(smem + kra[s & 7] + (s >> 3) * 256 + buf * 32768);
      s_ = MFMA32(kf, qf[s], s_);
    }
    return s_;
  };

  WAIT_VM0();
  BARRIER();
  f32x16 sc_cur = qk(0);

  for (int j = 0; j < 16; ++j) {
    WAIT_VM0();
    WAIT_LGKM0();
    BARRIER();
    if (j + 2 < 16) stageK(j + 2);
    if (j + 1 < 16) stageV(j + 1);
    f32x16 sc_nxt;
    if (j + 1 < 16) sc_nxt = qk((j + 1) & 1);
    float pmax = sc_cur[0];
#pragma unroll
    for (int r = 1; r < 16; ++r) pmax = fmaxf(pmax, sc_cur[r]);
    {
      auto rr = __builtin_amdgcn_permlane32_swap(__float_as_uint(pmax), __float_as_uint(pmax), false, false);
      pmax = fmaxf(__uint_as_float(rr[0]), __uint_as_float(rr[1]));
    }
    if (!__all(pmax - m_run <= THR)) {
      const float mn = fmaxf(m_run, pmax);
      const float alpha = __builtin_amdgcn_exp2f((m_run - mn) * SC);
      m_run = mn;
      l_run *= alpha;
#pragma unroll
      for (int t = 0; t < 8; ++t)
#pragma unroll
        for (int r = 0; r < 16; ++r) o[t][r] *= alpha;
    }
    const float mc = -m_run * SC;
    float ps = 0.f;
#pragma unroll
    for (int r = 0; r < 16; ++r) {
      sc_cur[r] = __builtin_amdgcn_exp2f(fmaf(sc_cur[r], SC, mc));
      ps += sc_cur[r];
    }
    l_run += ps;
    bf16x8 pf[2];
#pragma unroll
    for (int s = 0; s < 2; ++s) {
      u32x4 u;
#pragma unroll
      for (int e = 0; e < 4; ++e) u[e] = cvtpk(sc_cur[8 * s + 2 * e], sc_cur[8 * s + 2 * e + 1]);
      pf[s] = __builtin_bit_cast(bf16x8, u);
    }
    const char* vb = smem + (j & 1) * 32768;
#pragma unroll
    for (int t = 0; t < 8; ++t) {
#pragma unroll
      for (int s = 0; s < 2; ++s) {
        s16x4 lo = tr_read(vb + vra[t & 3] + (t >> 2) * 256 + s * 8192);
        s16x4 hi4 = tr_read(vb + vra[t & 3] + (t >> 2) * 256 + s * 8192 + 4096);
        o[t] = MFMA32(cat8(lo, hi4), pf[s], o[t]);
      }
    }
    sc_cur = sc_nxt;
  }

  WAIT_LGKM0();
  BARRIER();
  {
    auto rr = __builtin_amdgcn_permlane32_swap(__float_as_uint(l_run), __float_as_uint(l_run), false, false);
    l_run = __uint_as_float(rr[0]) + __uint_as_float(rr[1]);
  }
  float* xo = reinterpret_cast<float*>(smem) + w * 4096;
  float* xml = reinterpret_cast<float*>(smem + LDS_V) + w * 128;
  {
    const int tb = kh ? 0 : 4;
#pragma unroll
    for (int t = 0; t < 4; ++t)
#pragma unroll
      for (int r = 0; r < 16; ++r) xo[(t * 16 + r) * 64 + lane] = kh ? o[t][r] : o[4 + t][r];
    (void)tb;
    xml[lane] = m_run;
    xml[64 + lane] = l_run;
  }
  WAIT_LGKM0();
  BARRIER();
  {
    const int pw = w ^ 2;
    const float* po = reinterpret_cast<const float*>(smem) + pw * 4096;
    const float* pml = reinterpret_cast<const float*>(smem + LDS_V) + pw * 128;
    const float m2 = pml[lane], l2 = pml[64 + lane];
    const float mn = fmaxf(m_run, m2);
    const float a1 = __builtin_amdgcn_exp2f((m_run - mn) * SC), a2 = __builtin_amdgcn_exp2f((m2 - mn) * SC);
    const float inv = 1.0f / (a1 * l_run + a2 * l2);
    const float f1 = a1 * inv, f2 = a2 * inv;
    const int head = kvh * 2 + qt;
    __hip_bfloat16* op = o_ws + ((long)b * SQ + quarter * QR + r32) * (HQ * DH) + head * DH + 4 * hi;
    const int t0 = kh ? 4 : 0;
#pragma unroll
    for (int t = 0; t < 4; ++t) {
#pragma unroll
      for (int gq = 0; gq < 4; ++gq) {
        float v[4];
#pragma unroll
        for (int e = 0; e < 4; ++e) {
          const float mine = kh ? o[4 + t][4 * gq + e] : o[t][4 * gq + e];
          v[e] = mine * f1 + po[(t * 16 + 4 * gq + e) * 64 + lane] * f2;
        }
        u32x2 u;
        u[0] = cvtpk(v[0], v[1]);
        u[1] = cvtpk(v[2], v[3]);
        if (r32 < QR) *reinterpret_cast<u32x2*>(op + 32 * (t0 + t) + 8 * gq) = u;
      }
    }
  }
}

__global__ __launch_bounds__(256, 1)
void oproj_kernel(const __hip_bfloat16* __restrict__ o_ws, const __hip_bfloat16* __restrict__ wo,
                  __hip_bfloat16* __restrict__ out) {
  __shared__ __attribute__((aligned(16))) char smem[65536];
  const int tid = threadIdx.x, w = tid >> 6, lane = tid & 63, r32 = lane & 31, hi = lane >> 5;
  const int i16 = lane & 15, q4 = i16 >> 2, p4 = i16 & 3, g = lane >> 4, hh = g >> 1, cb = g & 1;
  const int bm = blockIdx.x / 7, bn = blockIdx.x % 7;
  constexpr int KD = HQ * DH;
  char* stg = smem + w * 16384;
  const int srow = lane >> 3;
  const int sc = (lane & 7) ^ (((srow >> 1) & 1) << 2);
  const __hip_bfloat16* wsrc = wo + (long)(w * 1024 + srow) * DM + bn * 64 + 8 * sc;
  auto stage = [&](int c) {
#pragma unroll
    for (int p = 0; p < 8; ++p) glds16(wsrc + (long)(64 * c + 8 * p) * DM, stg + (c & 1) * 8192 + p * 1024);
  };
  const __hip_bfloat16* ap = o_ws + (long)(bm * 32 + r32) * KD + w * 1024 + 8 * hi;
  f32x16 acc[2];
#pragma unroll
  for (int a = 0; a < 2; ++a)
#pragma unroll
    for (int r = 0; r < 16; ++r) acc[a][r] = 0.f;
  int tra[2];
#pragma unroll
  for (int a = 0; a < 2; ++a)
    tra[a] = (8 * hh + q4) * 128 + 16 * ((4 * a + 2 * cb + (p4 >> 1)) ^ (((q4 >> 1) & 1) << 2)) + 8 * (p4 & 1);
  stage(0);
  for (int c = 0; c < 16; ++c) {
    bf16x8 af[4];
#pragma unroll
    for (int s = 0; s < 4; ++s) af[s] = *reinterpret_cast<const bf16x8*>(ap + 64 * c + 16 * s);
    WAIT_VM0();
    if (c + 1 < 16) stage(c + 1);
    const char* src = stg + (c & 1) * 8192;
#pragma unroll
    for (int s = 0; s < 4; ++s)
#pragma unroll
      for (int a = 0; a < 2; ++a) {
        s16x4 lo = tr_read(src + tra[a] + s * 2048);
        s16x4 hi4 = tr_read(src + tra[a] + s * 2048 + 512);
        acc[a] = MFMA32(af[s], cat8(lo, hi4), acc[a]);
      }
    WAIT_LGKM0();
  }
  BARRIER();
  float* red = reinterpret_cast<float*>(smem);
#pragma unroll
  for (int a = 0; a < 2; ++a)
#pragma unroll
    for (int r = 0; r < 16; ++r) red[((w * 2 + a) * 16 + r) * 64 + lane] = acc[a][r];
  WAIT_LGKM0();
  BARRIER();
  {
    const int a = w >> 1, r0 = 8 * (w & 1);
#pragma unroll
    for (int r = 0; r < 8; ++r) {
      float v = 0.f;
#pragma unroll
      for (int ww = 0; ww < 4; ++ww) v += red[((ww * 2 + a) * 16 + r0 + r) * 64 + lane];
      const int rr = r0 + r;
      const int row = (rr & 3) + 8 * (rr >> 2) + 4 * hi;
      out[(long)(bm * 32 + row) * DM + bn * 64 + a * 32 + r32] = __float2bfloat16(v);
    }
  }
}

extern "C" void kernel_launch(void* const* d_in, const int* in_sizes, int n_in,
                              void* d_out, int out_size, void* d_ws, size_t ws_size,
                              hipStream_t stream) {
  const __hip_bfloat16* x = (const __hip_bfloat16*)d_in[0];
  const __hip_bfloat16* kc = (const __hip_bfloat16*)d_in[1];
  const __hip_bfloat16* vc = (const __hip_bfloat16*)d_in[2];
  const __hip_bfloat16* wq = (const __hip_bfloat16*)d_in[3];
  const __hip_bfloat16* wo = (const __hip_bfloat16*)d_in[4];
  __hip_bfloat16* o_ws = (__hip_bfloat16*)d_ws;
  __hip_bfloat16* out = (__hip_bfloat16*)d_out;
  attn_kernel<<<256, 256, 0, stream>>>(x, kc, vc, wq, o_ws);
  oproj_kernel<<<28 * 7, 256, 0, stream>>>(o_ws, wo, out);
}
```

```cpp
#include <hip/hip_runtime.h>
#include <hip/hip_bf16.h>
#include <stdint.h>

typedef short bf16x8 __attribute__((ext_vector_type(8)));
typedef short s16x4 __attribute__((ext_vector_type(4)));
typedef float f32x16 __attribute__((ext_vector_type(16)));
typedef float f32x4 __attribute__((ext_vector_type(4)));
typedef unsigned u32x4 __attribute__((ext_vector_type(4)));
typedef unsigned u32x2 __attribute__((ext_vector_type(2)));
typedef __attribute__((address_space(3))) s16x4* lds_s16x4_ptr;

constexpr int NB = 8, SQ = 112, DM = 448, HQ = 16, HKV = 8, SK = 1024, DH = 256;
constexpr int QR = 56;

#define MFMA32(a, b, c) __builtin_amdgcn_mfma_f32_32x32x16_bf16((a), (b), (c), 0, 0, 0)
#define DI __device__ __forceinline__
#define SBAR() __builtin_amdgcn_sched_barrier(0)

typedef __bf16 bf16x2_t __attribute__((ext_vector_type(2)));
typedef float f32x2 __attribute__((ext_vector_type(2)));
DI unsigned cvtpk(float lo, float hi) {
  f32x2 v = {lo, hi};
  return __builtin_bit_cast(unsigned, __builtin_convertvector(v, bf16x2_t));
}
DI s16x4 tr_read(const char* p) {
  return __builtin_amdgcn_ds_read_tr16_b64_v4i16((lds_s16x4_ptr)(p));
}
#define GLDS8_BODY(NT)                                     \
      "s_mov_b32 %0, m0\n\t"                               \
      "s_mov_b32 m0, %10\n\ts_nop 0\n\t"                   \
      "global_load_lds_dwordx4 %2, %1" NT "\n\t"           \
      "s_add_u32 m0, m0, 0x400\n\ts_nop 0\n\t"             \
      "global_load_lds_dwordx4 %3, %1" NT "\n\t"           \
      "s_add_u32 m0, m0, 0x400\n\ts_nop 0\n\t"             \
      "global_load_lds_dwordx4 %4, %1" NT "\n\t"           \
      "s_add_u32 m0, m0, 0x400\n\ts_nop 0\n\t"             \
      "global_load_lds_dwordx4 %5, %1" NT "\n\t"           \
      "s_add_u32 m0, m0, 0x3400\n\ts_nop 0\n\t"            \
      "global_load_lds_dwordx4 %6, %1" NT "\n\t"           \
      "s_add_u32 m0, m0, 0x400\n\ts_nop 0\n\t"             \
      "global_load_lds_dwordx4 %7, %1" NT "\n\t"           \
      "s_add_u32 m0, m0, 0x400\n\ts_nop 0\n\t"             \
      "global_load_lds_dwordx4 %8, %1" NT "\n\t"           \
      "s_add_u32 m0, m0, 0x400\n\ts_nop 0\n\t"             \
      "global_load_lds_dwordx4 %9, %1" NT "\n\t"           \
      "s_mov_b32 m0, %0"
DI void glds8(const void* sbase, unsigned dst, int o0, int o1, int o2, int o3, int o4, int o5, int o6, int o7) {
  unsigned keep;
  asm volatile(GLDS8_BODY("")
      : "=&s"(keep)
      : "s"(sbase), "v"(o0), "v"(o1), "v"(o2), "v"(o3), "v"(o4), "v"(o5), "v"(o6), "v"(o7), "s"(dst)
      : "memory");
}
DI void glds8nt(const void* sbase, unsigned dst, int o0, int o1, int o2, int o3, int o4, int o5, int o6, int o7) {
  unsigned keep;
  asm volatile(GLDS8_BODY(" nt")
      : "=&s"(keep)
      : "s"(sbase), "v"(o0), "v"(o1), "v"(o2), "v"(o3), "v"(o4), "v"(o5), "v"(o6), "v"(o7), "s"(dst)
      : "memory");
}
DI bf16x8 cat8(s16x4 lo, s16x4 hi) { return __builtin_shufflevector(lo, hi, 0, 1, 2, 3, 4, 5, 6, 7); }

#define WAIT_VM(n) asm volatile("s_waitcnt vmcnt(" #n ")" ::: "memory")
#define WAIT_LGKM0() asm volatile("s_waitcnt lgkmcnt(0)" ::: "memory")
#define BARRIER() do { asm volatile("" ::: "memory"); __builtin_amdgcn_s_barrier(); asm volatile("" ::: "memory"); } while (0)
#define GLDS_KV glds8nt

constexpr int LDS_IMG = 131072;
constexpr int LDS_TOTAL = 163840;
constexpr int NSTAGE = 47;

__global__ __launch_bounds__(256, 1)
void attn_kernel(const __hip_bfloat16* __restrict__ x, const __hip_bfloat16* __restrict__ kc,
                 const __hip_bfloat16* __restrict__ vc, const __hip_bfloat16* __restrict__ wq,
                 const __hip_bfloat16* __restrict__ wo, float* __restrict__ part) {
  __shared__ __attribute__((aligned(16))) char smem[LDS_TOTAL];
  const int tid = threadIdx.x, lane = tid & 63, r32 = lane & 31, hi = lane >> 5;
  const int w = __builtin_amdgcn_readfirstlane(tid >> 6);
  const int i16 = lane & 15, q4 = i16 >> 2, p4 = i16 & 3, g = lane >> 4, hh = g >> 1, cb = g & 1;
  const unsigned lds0 = __builtin_amdgcn_readfirstlane((unsigned)(uintptr_t)smem);
  const int bid = blockIdx.x;
  const int b = bid & 7, rest = bid >> 3, kvh = rest >> 2, head = rest >> 1, half = rest & 1;
  const int qt = w & 1, kh = w >> 1;
#define ST()
  ST();

  int offs_s[2];
  int offs_k[4];
#pragma unroll
  for (int p = 0; p < 4; ++p) {
    const int row = 8 * w + 2 * p + hi;
    offs_k[p] = row * 512 + 16 * (r32 ^ (row & 15));
    if (p < 2) offs_s[p] = row * 512 + 16 * (r32 ^ ((row & 3) << 2));
  }
  const char* kg = (const char*)(kc + (long)(b * HKV + kvh) * SK * DH);
  const char* vg = (const char*)(vc + (long)(b * HKV + kvh) * SK * DH);
  const char* wqg = (const char*)(wq + head * DH);
  const char* wog = (const char*)(wo + (long)head * DH * DM);
  auto issue = [&](int s) {
    const unsigned dst = lds0 + (s & 3) * 32768 + w * 4096;
    if (s < 7) {
      int o[4];
#pragma unroll
      for (int p = 0; p < 4; ++p) {
        const int row = 8 * w + 2 * p + hi;
        o[p] = row * 8192 + 16 * (r32 ^ ((row & 3) << 2));
      }
      glds8(wqg + (long)s * 64 * 8192, dst, o[0], o[1], o[2], o[3], o[0] + 262144, o[1] + 262144, o[2] + 262144, o[3] + 262144);
    } else if (s < 39) {
      const bool isK = (s == 7) || ((s & 1) == 0 && s != 38);
      if (isK) {
        const int j = (s == 7) ? 0 : (s - 6) >> 1;
        GLDS_KV(kg + (long)j * 16384, dst, offs_k[0], offs_k[1], offs_k[2], offs_k[3], offs_k[0] + 262144, offs_k[1] + 262144,
                offs_k[2] + 262144, offs_k[3] + 262144);
      } else {
        const int j = (s == 38) ? 15 : (s - 9) >> 1;
        GLDS_KV(vg + (long)j * 16384, dst, offs_s[0], offs_s[1], offs_s[0] + 2048, offs_s[1] + 2048, offs_s[0] + 262144,
                offs_s[1] + 262144, offs_s[0] + 264192, offs_s[1] + 264192);
      }
    } else {
      const int c = s - 39;
      int o[8];
#pragma unroll
      for (int i = 0; i < 8; ++i) {
        const int v = (2 * c + (i >> 2)) * 14336 + (4 * w + (i & 3)) * 1024 + lane * 16;
        o[i] = v < DH * DM * 2 - 16 ? v : DH * DM * 2 - 16;
      }
      glds8(wog, dst, o[0], o[1], o[2], o[3], o[4], o[5], o[6], o[7]);
    }
  };
#define STAGE_ENTER(s) do { WAIT_VM(16); WAIT_LGKM0(); BARRIER(); if ((s) + 3 < NSTAGE) issue((s) + 3); } while (0)
#define STAGE_ENTER_TAIL(s, n) do { WAIT_VM(n); WAIT_LGKM0(); BARRIER(); } while (0)

  bf16x8 xf[2][28];
#pragma unroll
  for (int t = 0; t < 2; ++t) {
    const int rr = 32 * t + r32;
    const int xr = half * QR + (rr < QR ? rr : QR - 1);
    const __hip_bfloat16* xp = x + ((long)b * SQ + xr) * DM + 8 * hi;
#pragma unroll
    for (int s = 0; s < 28; ++s) xf[t][s] = *reinterpret_cast<const bf16x8*>(xp + 16 * s);
  }
  issue(0);
  issue(1);
  issue(2);

  {
    f32x16 qacc[2][2];
#pragma unroll
    for (int a = 0; a < 2; ++a)
#pragma unroll
      for (int t = 0; t < 2; ++t)
#pragma unroll
        for (int r = 0; r < 16; ++r) qacc[a][t][r] = 0.f;
    int tra[2];
#pragma unroll
    for (int a = 0; a < 2; ++a)
      tra[a] = (8 * hh + q4) * 512 + 16 * ((8 * w + 4 * a + 2 * cb + (p4 >> 1)) ^ (q4 << 2)) + 8 * (p4 & 1);
#pragma unroll
    for (int c = 0; c < 7; ++c) {
      STAGE_ENTER(c);
      const char* src = smem + (c & 3) * 32768;
#pragma unroll
      for (int s = 0; s < 4; ++s) {
#pragma unroll
        for (int a = 0; a < 2; ++a) {
          const bf16x8 af = cat8(tr_read(src + tra[a] + s * 8192), tr_read(src + tra[a] + s * 8192 + 2048));
          qacc[a][0] = MFMA32(af, xf[0][4 * c + s], qacc[a][0]);
          qacc[a][1] = MFMA32(af, xf[1][4 * c + s], qacc[a][1]);
        }
      }
    }
    ST();
#pragma unroll
    for (int a = 0; a < 2; ++a)
#pragma unroll
      for (int t = 0; t < 2; ++t)
#pragma unroll
        for (int gq = 0; gq < 4; ++gq) {
          u32x2 v;
          v[0] = cvtpk(qacc[a][t][4 * gq + 0], qacc[a][t][4 * gq + 1]);
          v[1] = cvtpk(qacc[a][t][4 * gq + 2], qacc[a][t][4 * gq + 3]);
          const int row = 32 * t + r32, ch = 8 * w + 4 * a + gq;
          *reinterpret_cast<u32x2*>(smem + LDS_IMG + row * 512 + 16 * (ch ^ (row & 15)) + 8 * hi) = v;
        }
  }
  WAIT_LGKM0();
  BARRIER();
  bf16x8 qf[16];
  {
    const int row = 32 * qt + r32;
#pragma unroll
    for (int s = 0; s < 16; ++s)
      qf[s] = *reinterpret_cast<const bf16x8*>(smem + LDS_IMG + row * 512 + 16 * ((2 * s + hi) ^ (row & 15)));
  }

  int kra[8];
#pragma unroll
  for (int s = 0; s < 8; ++s) kra[s] = kh * 16384 + r32 * 512 + 16 * ((2 * s + hi) ^ (r32 & 15));
  int vra[4];
#pragma unroll
  for (int t = 0; t < 4; ++t)
    vra[t] = kh * 16384 + (4 * hh + q4) * 512 + 16 * (4 * (t ^ q4) + 2 * cb + (p4 >> 1)) + 8 * (p4 & 1);

  f32x16 o[8];
#pragma unroll
  for (int t = 0; t < 8; ++t)
#pragma unroll
    for (int r = 0; r < 16; ++r) o[t][r] = 0.f;
  float m_run = -1e30f, l_run = 0.f;
  constexpr float SC = 0.0625f * 1.4426950408889634f;
  constexpr float THR = 8.0f / 0.0625f;

#define KFRAG(base, s) (*reinterpret_cast<const bf16x8*>((base) + kra[(s) & 7] + ((s) >> 3) * 256))
  STAGE_ENTER(7);
  f32x16 sc_cur;
  {
    const char* kb = smem + (7 & 3) * 32768;
#pragma unroll
    for (int r = 0; r < 16; ++r) sc_cur[r] = 0.f;
#pragma unroll
    for (int s = 0; s < 16; ++s) sc_cur = MFMA32(KFRAG(kb, s), qf[s], sc_cur);
  }
  ST();

  for (int j = 0; j < 16; ++j) {
    const int sA = 8 + 2 * j;
    if (j < 15) STAGE_ENTER(sA);
    const char* kb = smem + (sA & 3) * 32768;
    f32x16 sc_nxt;
#pragma unroll
    for (int r = 0; r < 16; ++r) sc_nxt[r] = 0.f;
    constexpr int KD_ = 6;
    bf16x8 kf[KD_];
#pragma unroll
    for (int s = 0; s < KD_ - 1; ++s) kf[s] = KFRAG(kb, s);
    SBAR();
    kf[KD_ - 1] = KFRAG(kb, KD_ - 1);
    sc_nxt = MFMA32(kf[0], qf[0], sc_nxt);
    float pm0 = fmaxf(fmaxf(sc_cur[0], sc_cur[1]), sc_cur[2]);
    pm0 = fmaxf(fmaxf(pm0, sc_cur[3]), sc_cur[4]);
    float pm1 = fmaxf(fmaxf(sc_cur[5], sc_cur[6]), sc_cur[7]);
    pm1 = fmaxf(fmaxf(pm1, sc_cur[8]), sc_cur[9]);
    SBAR();
    kf[0] = KFRAG(kb, KD_);
    sc_nxt = MFMA32(kf[1], qf[1], sc_nxt);
    pm0 = fmaxf(fmaxf(pm0, sc_cur[10]), sc_cur[11]);
    pm1 = fmaxf(fmaxf(pm1, sc_cur[12]), sc_cur[13]);
    pm0 = fmaxf(fmaxf(pm0, sc_cur[14]), sc_cur[15]);
    float pmax = fmaxf(pm0, pm1);
    SBAR();
    kf[1] = KFRAG(kb, KD_ + 1);
    sc_nxt = MFMA32(kf[2], qf[2], sc_nxt);
    {
      auto rr = __builtin_amdgcn_permlane32_swap(__float_as_uint(pmax), __float_as_uint(pmax), false, false);
      pmax = fmaxf(__uint_as_float(rr[0]), __uint_as_float(rr[1]));
    }
    SBAR();
    if (!__all(pmax - m_run <= THR)) {
      const float mn = fmaxf(m_run, pmax);
      const float alpha = __builtin_amdgcn_exp2f((m_run - mn) * SC);
      m_run = mn;
      l_run *= alpha;
      asm volatile("s_nop 7\n\ts_nop 7\n\ts_nop 3" ::: "memory");
#pragma unroll
      for (int t = 0; t < 8; ++t)
#pragma unroll
        for (int r = 0; r < 16; ++r) {
          float tmp;
          asm volatile("v_accvgpr_read_b32 %1, %0\n\ts_nop 0\n\tv_mul_f32 %1, %1, %2\n\ts_nop 0\n\tv_accvgpr_write_b32 %0, %1"
                       : "+a"(o[t][r]), "=&v"(tmp) : "v"(alpha));
        }
      asm volatile("s_nop 3" ::: "memory");
    }
    const float mc = -m_run * SC;
    unsigned pu[8];
#pragma unroll
    for (int s = 3; s < 16; ++s) {
      if (s + KD_ - 1 < 16) kf[(s + KD_ - 1) % KD_] = KFRAG(kb, s + KD_ - 1);
      sc_nxt = MFMA32(kf[s % KD_], qf[s], sc_nxt);
#pragma unroll
      for (int e = 0; e < 16; ++e) {
        if (3 + (e * 12) / 16 == s) {
          sc_cur[e] = __builtin_amdgcn_exp2f(fmaf(sc_cur[e], SC, mc));
          if (e & 1) pu[e >> 1] = cvtpk(sc_cur[e - 1], sc_cur[e]);
        }
      }
      SBAR();
    }
    bf16x8 pf[2];
    {
      u32x4 u0 = {pu[0], pu[1], pu[2], pu[3]}, u1 = {pu[4], pu[5], pu[6], pu[7]};
      pf[0] = __builtin_bit_cast(bf16x8, u0);
      pf[1] = __builtin_bit_cast(bf16x8, u1);
    }
    const int sB = (j < 15) ? 9 + 2 * j : 38;
    STAGE_ENTER(sB);
    const char* vb = smem + (sB & 3) * 32768;
#define VFRAG(i) cat8(tr_read(vb + vra[(i) & 3] + (((i) >> 2) & 1) * 256 + ((i) >> 3) * 8192), \
                      tr_read(vb + vra[(i) & 3] + (((i) >> 2) & 1) * 256 + ((i) >> 3) * 8192 + 4096))
    constexpr int VD_ = 6;
    bf16x8 vf[VD_];
#pragma unroll
    for (int i = 0; i < VD_ - 1; ++i) vf[i] = VFRAG(i);
    SBAR();
    float ps0 = 0.f, ps1 = 0.f;
#pragma unroll
    for (int i = 0; i < 16; ++i) {
      if (i + VD_ - 1 < 16) vf[(i + VD_ - 1) % VD_] = VFRAG(i + VD_ - 1);
      o[i & 7] = MFMA32(vf[i % VD_], pf[i >> 3], o[i & 7]);
      if (i & 1) ps1 += sc_cur[i]; else ps0 += sc_cur[i];
      SBAR();
    }
    l_run += ps0 + ps1;
    sc_cur = sc_nxt;
    ST();
  }

  {
    auto rr = __builtin_amdgcn_permlane32_swap(__float_as_uint(l_run), __float_as_uint(l_run), false, false);
    l_run = __uint_as_float(rr[0]) + __uint_as_float(rr[1]);
  }
  float f1, f2;
  {
    float* xo = reinterpret_cast<float*>(smem + LDS_IMG) + w * 2048;
    const float* po = reinterpret_cast<const float*>(smem + LDS_IMG) + (w ^ 2) * 2048;
    xo[lane] = m_run;
    xo[64 + lane] = l_run;
    WAIT_LGKM0();
    BARRIER();
    const float m2 = po[lane], l2 = po[64 + lane];
    const float mn = fmaxf(m_run, m2);
    const float a1 = __builtin_amdgcn_exp2f((m_run - mn) * SC), a2 = __builtin_amdgcn_exp2f((m2 - mn) * SC);
    const float inv = 1.0f / (a1 * l_run + a2 * l2);
    f1 = a1 * inv;
    f2 = a2 * inv;
    WAIT_LGKM0();
    BARRIER();
#pragma unroll
    for (int rd = 0; rd < 2; ++rd) {
#pragma unroll
      for (int t = 0; t < 2; ++t)
#pragma unroll
        for (int r = 0; r < 16; ++r) xo[(t * 16 + r) * 64 + lane] = kh ? o[2 * rd + t][r] : o[4 + 2 * rd + t][r];
      WAIT_LGKM0();
      BARRIER();
#pragma unroll
      for (int t = 0; t < 2; ++t)
#pragma unroll
        for (int r = 0; r < 16; ++r) {
          const float pv = po[(t * 16 + r) * 64 + lane];
          if (kh) o[4 + 2 * rd + t][r] = o[4 + 2 * rd + t][r] * f1 + pv * f2;
          else o[2 * rd + t][r] = o[2 * rd + t][r] * f1 + pv * f2;
        }
      WAIT_LGKM0();
      BARRIER();
    }
#pragma unroll
    for (int t = 0; t < 4; ++t)
#pragma unroll
      for (int gq = 0; gq < 4; ++gq) {
        u32x2 v;
        if (kh) {
          v[0] = cvtpk(o[4 + t][4 * gq + 0], o[4 + t][4 * gq + 1]);
          v[1] = cvtpk(o[4 + t][4 * gq + 2], o[4 + t][4 * gq + 3]);
        } else {
          v[0] = cvtpk(o[t][4 * gq + 0], o[t][4 * gq + 1]);
          v[1] = cvtpk(o[t][4 * gq + 2], o[t][4 * gq + 3]);
        }
        const int row = 32 * qt + r32, ch = 16 * kh + 4 * t + gq;
        *reinterpret_cast<u32x2*>(smem + LDS_IMG + row * 512 + 16 * (ch ^ (row & 15)) + 8 * hi) = v;
      }
  }
  ST();

  {
    f32x16 acc[7];
#pragma unroll
    for (int u = 0; u < 7; ++u)
#pragma unroll
      for (int r = 0; r < 16; ++r) acc[u][r] = 0.f;
    const int tro = (8 * hh + q4) * 896 + 32 * cb + 8 * p4;
    const int nt3 = 12 + (w >> 1);
#pragma unroll
    for (int c = 0; c < 8; ++c) {
      if (c < 5) STAGE_ENTER(39 + c);
      else if (c == 5) STAGE_ENTER_TAIL(39 + c, 16);
      else if (c == 6) STAGE_ENTER_TAIL(39 + c, 8);
      else STAGE_ENTER_TAIL(39 + c, 0);
      const char* src = smem + ((39 + c) & 3) * 32768;
#pragma unroll
      for (int f = 0; f < 2; ++f) {
        const int ks = 2 * c + f;
        bf16x8 bq[2];
#pragma unroll
        for (int t = 0; t < 2; ++t) {
          const int row = 32 * t + r32;
          bq[t] = *reinterpret_cast<const bf16x8*>(smem + LDS_IMG + row * 512 + 16 * ((2 * ks + hi) ^ (row & 15)));
        }
        const char* sf = src + f * 16384 + tro;
#pragma unroll
        for (int u = 0; u < 3; ++u) {
          const bf16x8 af = cat8(tr_read(sf + 64 * (w + 4 * u)), tr_read(sf + 64 * (w + 4 * u) + 4 * 896));
          acc[2 * u] = MFMA32(af, bq[0], acc[2 * u]);
          acc[2 * u + 1] = MFMA32(af, bq[1], acc[2 * u + 1]);
        }
        {
          const bf16x8 af = cat8(tr_read(sf + 64 * nt3), tr_read(sf + 64 * nt3 + 4 * 896));
          acc[6] = MFMA32(af, qt ? bq[1] : bq[0], acc[6]);
        }
      }
    }
    ST();
    float* pp = part + ((long)head * (NB * SQ) + b * SQ + half * QR) * DM + 4 * hi;
#pragma unroll
    for (int u = 0; u < 7; ++u) {
      const int nt = (u < 6) ? (w + 4 * (u >> 1)) : nt3;
      const int t = (u < 6) ? (u & 1) : qt;
      const int row = 32 * t + r32;
      if (row < QR) {
#pragma unroll
        for (int gq = 0; gq < 4; ++gq) {
          f32x4 v = {acc[u][4 * gq], acc[u][4 * gq + 1], acc[u][4 * gq + 2], acc[u][4 * gq + 3]};
          *reinterpret_cast<f32x4*>(pp + (long)row * DM + 32 * nt + 8 * gq) = v;
        }
      }
    }
  }
  ST();
}

__global__ __launch_bounds__(256)
void reduce_kernel(const float* __restrict__ part, __hip_bfloat16* __restrict__ out) {
  const int i = blockIdx.x * 256 + threadIdx.x;
  constexpr int N4 = NB * SQ * DM / 4;
  const f32x4* p = reinterpret_cast<const f32x4*>(part) + i;
  f32x4 v[HQ];
#pragma unroll
  for (int h = 0; h < HQ; ++h) v[h] = __builtin_nontemporal_load(p + (long)h * N4);
  f32x4 s = v[0];
#pragma unroll
  for (int h = 1; h < HQ; ++h) s += v[h];
  u32x2 u;
  u[0] = cvtpk(s[0], s[1]);
  u[1] = cvtpk(s[2], s[3]);
  *reinterpret_cast<u32x2*>(out + (long)i * 4) = u;
}

extern "C" void kernel_launch(void* const* d_in, const int* in_sizes, int n_in,
                              void* d_out, int out_size, void* d_ws, size_t ws_size,
                              hipStream_t stream) {
  const __hip_bfloat16* x = (const __hip_bfloat16*)d_in[0];
  const __hip_bfloat16* kc = (const __hip_bfloat16*)d_in[1];
  const __hip_bfloat16* vc = (const __hip_bfloat16*)d_in[2];
  const __hip_bfloat16* wq = (const __hip_bfloat16*)d_in[3];
  const __hip_bfloat16* wo = (const __hip_bfloat16*)d_in[4];
  float* part = (float*)d_ws;
  __hip_bfloat16* out = (__hip_bfloat16*)d_out;
  attn_kernel<<<256, 256, 0, stream>>>(x, kc, vc, wq, wo, part);
  reduce_kernel<<<NB * SQ * DM / 4 / 256, 256, 0, stream>>>(part, out);
}
```

```cpp
#include <hip/hip_runtime.h>
#include <hip/hip_bf16.h>
#include <stdint.h>
#include <type_traits>

typedef short bf16x8 __attribute__((ext_vector_type(8)));
typedef short s16x4 __attribute__((ext_vector_type(4)));
typedef float f32x16 __attribute__((ext_vector_type(16)));
typedef float f32x4 __attribute__((ext_vector_type(4)));
typedef unsigned u32x4 __attribute__((ext_vector_type(4)));
typedef unsigned u32x2 __attribute__((ext_vector_type(2)));
typedef __attribute__((address_space(3))) s16x4* lds_s16x4_ptr;

constexpr int NB = 8, SQ = 112, DM = 448, HQ = 16, HKV = 8, SK = 1024, DH = 256;
constexpr int QR = 56;

#define MFMA32(a, b, c) __builtin_amdgcn_mfma_f32_32x32x16_bf16((a), (b), (c), 0, 0, 0)
#define DI __device__ __forceinline__
#define SBAR() __builtin_amdgcn_sched_barrier(0)

typedef __bf16 bf16x2_t __attribute__((ext_vector_type(2)));
typedef float f32x2 __attribute__((ext_vector_type(2)));
DI unsigned cvtpk(float lo, float hi) {
  f32x2 v = {lo, hi};
  return __builtin_bit_cast(unsigned, __builtin_convertvector(v, bf16x2_t));
}
DI s16x4 tr_read(const char* p) {
  return __builtin_amdgcn_ds_read_tr16_b64_v4i16((lds_s16x4_ptr)(p));
}
#define GLDS8_BODY(NT)                                     \
      "s_mov_b32 %0, m0\n\t"                               \
      "s_mov_b32 m0, %10\n\ts_nop 0\n\t"                   \
      "global_load_lds_dwordx4 %2, %1" NT "\n\t"           \
      "s_add_u32 m0, m0, 0x400\n\ts_nop 0\n\t"             \
      "global_load_lds_dwordx4 %3, %1" NT "\n\t"           \
      "s_add_u32 m0, m0, 0x400\n\ts_nop 0\n\t"             \
      "global_load_lds_dwordx4 %4, %1" NT "\n\t"           \
      "s_add_u32 m0, m0, 0x400\n\ts_nop 0\n\t"             \
      "global_load_lds_dwordx4 %5, %1" NT "\n\t"           \
      "s_add_u32 m0, m0, 0x3400\n\ts_nop 0\n\t"            \
      "global_load_lds_dwordx4 %6, %1" NT "\n\t"           \
      "s_add_u32 m0, m0, 0x400\n\ts_nop 0\n\t"             \
      "global_load_lds_dwordx4 %7, %1" NT "\n\t"           \
      "s_add_u32 m0, m0, 0x400\n\ts_nop 0\n\t"             \
      "global_load_lds_dwordx4 %8, %1" NT "\n\t"           \
      "s_add_u32 m0, m0, 0x400\n\ts_nop 0\n\t"             \
      "global_load_lds_dwordx4 %9, %1" NT "\n\t"           \
      "s_mov_b32 m0, %0"
DI void glds8(const void* sbase, unsigned dst, int o0, int o1, int o2, int o3, int o4, int o5, int o6, int o7) {
  unsigned keep;
  asm volatile(GLDS8_BODY("")
      : "=&s"(keep)
      : "s"(sbase), "v"(o0), "v"(o1), "v"(o2), "v"(o3), "v"(o4), "v"(o5), "v"(o6), "v"(o7), "s"(dst)
      : "memory");
}
DI void glds8nt(const void* sbase, unsigned dst, int o0, int o1, int o2, int o3, int o4, int o5, int o6, int o7) {
  unsigned keep;
  asm volatile(GLDS8_BODY(" nt")
      : "=&s"(keep)
      : "s"(sbase), "v"(o0), "v"(o1), "v"(o2), "v"(o3), "v"(o4), "v"(o5), "v"(o6), "v"(o7), "s"(dst)
      : "memory");
}
DI void glds1(const void* sbase, unsigned dst, int o0) {
  unsigned keep;
  asm volatile("s_mov_b32 %0, m0\n\ts_mov_b32 m0, %3\n\ts_nop 0\n\tglobal_load_lds_dwordx4 %2, %1\n\ts_mov_b32 m0, %0"
      : "=&s"(keep) : "s"(sbase), "v"(o0), "s"(dst) : "memory");
}
DI void glds1nt(const void* sbase, unsigned dst, int o0) {
  unsigned keep;
  asm volatile("s_mov_b32 %0, m0\n\ts_mov_b32 m0, %3\n\ts_nop 0\n\tglobal_load_lds_dwordx4 %2, %1 nt\n\ts_mov_b32 m0, %0"
      : "=&s"(keep) : "s"(sbase), "v"(o0), "s"(dst) : "memory");
}
DI bf16x8 cat8(s16x4 lo, s16x4 hi) { return __builtin_shufflevector(lo, hi, 0, 1, 2, 3, 4, 5, 6, 7); }

#define WAIT_VM(n) asm volatile("s_waitcnt vmcnt(" #n ")" ::: "memory")
#define WAIT_LGKM0() asm volatile("s_waitcnt lgkmcnt(0)" ::: "memory")
#define BARRIER() do { asm volatile("" ::: "memory"); __builtin_amdgcn_s_barrier(); asm volatile("" ::: "memory"); } while (0)
#define GLDS_KV glds8
#define GLDS_KV1 glds1

constexpr int LDS_IMG = 131072;
constexpr int LDS_TOTAL = 163840;
constexpr int NSTAGE = 47;

__global__ __launch_bounds__(256, 1)
void attn_kernel(const __hip_bfloat16* __restrict__ x, const __hip_bfloat16* __restrict__ kc,
                 const __hip_bfloat16* __restrict__ vc, const __hip_bfloat16* __restrict__ wq,
                 const __hip_bfloat16* __restrict__ wo, float* __restrict__ part) {
  __shared__ __attribute__((aligned(16))) char smem[LDS_TOTAL];
  const int tid = threadIdx.x, lane = tid & 63, r32 = lane & 31, hi = lane >> 5;
  const int w = __builtin_amdgcn_readfirstlane(tid >> 6);
  const int i16 = lane & 15, q4 = i16 >> 2, p4 = i16 & 3, g = lane >> 4, hh = g >> 1, cb = g & 1;
  const unsigned lds0 = __builtin_amdgcn_readfirstlane((unsigned)(uintptr_t)smem);
  const int bid = blockIdx.x;
  const int b = bid & 7, rest = bid >> 3, kvh = rest >> 2, head = rest >> 1, half = rest & 1;
  const int qt = w & 1, kh = w >> 1;
#define ST()
  ST();

  int offs_s[2];
  int offs_k[4];
#pragma unroll
  for (int p = 0; p < 4; ++p) {
    const int row = 8 * w + 2 * p + hi;
    offs_k[p] = row * 512 + 16 * (r32 ^ (row & 15));
    if (p < 2) offs_s[p] = row * 512 + 16 * (r32 ^ ((row & 3) << 2));
  }
  const char* kg = (const char*)(kc + (long)(b * HKV + kvh) * SK * DH);
  const char* vg = (const char*)(vc + (long)(b * HKV + kvh) * SK * DH);
  const char* wqg = (const char*)(wq + head * DH);
  const char* wog = (const char*)(wo + (long)head * DH * DM);
  auto issue = [&](int s) {
    const unsigned dst = lds0 + (s & 3) * 32768 + w * 4096;
    if (s < 7) {
      int o[4];
#pragma unroll
      for (int p = 0; p < 4; ++p) {
        const int row = 8 * w + 2 * p + hi;
        o[p] = row * 8192 + 16 * (r32 ^ ((row & 3) << 2));
      }
      glds8(wqg + (long)s * 64 * 8192, dst, o[0], o[1], o[2], o[3], o[0] + 262144, o[1] + 262144, o[2] + 262144, o[3] + 262144);
    } else if (s < 39) {
      const bool isK = (s == 7) || ((s & 1) == 0 && s != 38);
      if (isK) {
        const int j = (s == 7) ? 0 : (s - 6) >> 1;
        GLDS_KV(kg + (long)j * 16384, dst, offs_k[0], offs_k[1], offs_k[2], offs_k[3], offs_k[0] + 262144, offs_k[1] + 262144,
                offs_k[2] + 262144, offs_k[3] + 262144);
      } else {
        const int j = (s == 38) ? 15 : (s - 9) >> 1;
        GLDS_KV(vg + (long)j * 16384, dst, offs_s[0], offs_s[1], offs_s[0] + 2048, offs_s[1] + 2048, offs_s[0] + 262144,
                offs_s[1] + 262144, offs_s[0] + 264192, offs_s[1] + 264192);
      }
    } else {
      const int c = s - 39;
      int o[8];
#pragma unroll
      for (int i = 0; i < 8; ++i) {
        const int v = (2 * c + (i >> 2)) * 14336 + (4 * w + (i & 3)) * 1024 + lane * 16;
        o[i] = v < DH * DM * 2 - 16 ? v : DH * DM * 2 - 16;
      }
      glds8(wog, dst, o[0], o[1], o[2], o[3], o[4], o[5], o[6], o[7]);
    }
  };
  auto issueK1 = [&](int j, int sl, int i) {
    GLDS_KV1(kg + (long)j * 16384, lds0 + sl * 32768 + w * 4096 + (i & 3) * 1024 + (i >> 2) * 16384, offs_k[i & 3] + (i >> 2) * 262144);
  };
  auto issueV1 = [&](int j, int sl, int i) {
    GLDS_KV1(vg + (long)j * 16384, lds0 + sl * 32768 + w * 4096 + (i & 3) * 1024 + (i >> 2) * 16384,
             offs_s[i & 1] + ((i >> 1) & 1) * 2048 + (i >> 2) * 262144);
  };
  auto issueW1 = [&](int c, int i) {
    const int v = (2 * c + (i >> 2)) * 14336 + (4 * w + (i & 3)) * 1024 + lane * 16;
    glds1(wog, lds0 + ((39 + c) & 3) * 32768 + w * 4096 + (i & 3) * 1024 + (i >> 2) * 16384, v < DH * DM * 2 - 16 ? v : DH * DM * 2 - 16);
  };
#define STAGE_ENTER_NOISSUE() do { WAIT_VM(16); WAIT_LGKM0(); BARRIER(); } while (0)
#define STAGE_ENTER(s) do { WAIT_VM(16); WAIT_LGKM0(); BARRIER(); if ((s) + 3 < NSTAGE) issue((s) + 3); } while (0)
#define STAGE_ENTER_TAIL(s, n) do { WAIT_VM(n); WAIT_LGKM0(); BARRIER(); } while (0)

  {
    const char* xg = (const char*)(x + ((long)b * SQ + half * QR) * DM);
#pragma unroll
    for (int i = 0; i < 13; ++i) {
      int pc = 4 * i + w;
      if (pc > 48) pc = 48;
      const int by = pc * 1024 + lane * 16;
      const int row = by / 896, cp = (by - row * 896) >> 4;
      glds1(xg, lds0 + 65536 + pc * 1024, row * 896 + 16 * (cp ^ ((row >> 1) & 7)));
    }
  }
  issue(0);
  issue(1);
  WAIT_VM(16);
  BARRIER();
  bf16x8 xf[2][28];
#pragma unroll
  for (int t = 0; t < 2; ++t) {
    const int rr = 32 * t + r32;
    const int row = rr < QR ? rr : QR - 1;
#pragma unroll
    for (int s = 0; s < 28; ++s)
      xf[t][s] = *reinterpret_cast<const bf16x8*>(smem + 65536 + row * 896 + 16 * ((2 * s + hi) ^ ((row >> 1) & 7)));
  }
  WAIT_LGKM0();
  BARRIER();
  issue(2);

  {
    f32x16 qacc[2][2];
#pragma unroll
    for (int a = 0; a < 2; ++a)
#pragma unroll
      for (int t = 0; t < 2; ++t)
#pragma unroll
        for (int r = 0; r < 16; ++r) qacc[a][t][r] = 0.f;
    int tra[2];
#pragma unroll
    for (int a = 0; a < 2; ++a)
      tra[a] = (8 * hh + q4) * 512 + 16 * ((8 * w + 4 * a + 2 * cb + (p4 >> 1)) ^ (q4 << 2)) + 8 * (p4 & 1);
#pragma unroll
    for (int c = 0; c < 7; ++c) {
      STAGE_ENTER(c);
      ST();
      const char* src = smem + (c & 3) * 32768;
#pragma unroll
      for (int s = 0; s < 4; ++s) {
#pragma unroll
        for (int a = 0; a < 2; ++a) {
          const bf16x8 af = cat8(tr_read(src + tra[a] + s * 8192), tr_read(src + tra[a] + s * 8192 + 2048));
          qacc[a][0] = MFMA32(af, xf[0][4 * c + s], qacc[a][0]);
          qacc[a][1] = MFMA32(af, xf[1][4 * c + s], qacc[a][1]);
        }
      }
    }
    ST();
#pragma unroll
    for (int a = 0; a < 2; ++a)
#pragma unroll
      for (int t = 0; t < 2; ++t)
#pragma unroll
        for (int gq = 0; gq < 4; ++gq) {
          u32x2 v;
          v[0] = cvtpk(qacc[a][t][4 * gq + 0], qacc[a][t][4 * gq + 1]);
          v[1] = cvtpk(qacc[a][t][4 * gq + 2], qacc[a][t][4 * gq + 3]);
          const int row = 32 * t + r32, ch = 8 * w + 4 * a + gq;
          *reinterpret_cast<u32x2*>(smem + LDS_IMG + row * 512 + 16 * (ch ^ (row & 15)) + 8 * hi) = v;
        }
  }
  WAIT_LGKM0();
  BARRIER();
  bf16x8 qf[16];
  {
    const int row = 32 * qt + r32;
#pragma unroll
    for (int s = 0; s < 16; ++s)
      qf[s] = *reinterpret_cast<const bf16x8*>(smem + LDS_IMG + row * 512 + 16 * ((2 * s + hi) ^ (row & 15)));
  }

  int kra[8];
#pragma unroll
  for (int s = 0; s < 8; ++s) kra[s] = kh * 16384 + r32 * 512 + 16 * ((2 * s + hi) ^ (r32 & 15));
  int vra[4];
#pragma unroll
  for (int t = 0; t < 4; ++t)
    vra[t] = kh * 16384 + (4 * hh + q4) * 512 + 16 * (4 * (t ^ q4) + 2 * cb + (p4 >> 1)) + 8 * (p4 & 1);

  f32x16 o[8];
#pragma unroll
  for (int t = 0; t < 8; ++t)
#pragma unroll
    for (int r = 0; r < 16; ++r) o[t][r] = 0.f;
  float m_run = -1e30f, l_run = 0.f;
  constexpr float SC = 0.0625f * 1.4426950408889634f;
  constexpr float THR = 8.0f / 0.0625f;

#define KFRAG(base, s) (*reinterpret_cast<const bf16x8*>((base) + kra[(s) & 7] + ((s) >> 3) * 256))
  STAGE_ENTER(7);
  f32x16 sc_cur;
  {
    const char* kb = smem + (7 & 3) * 32768;
#pragma unroll
    for (int r = 0; r < 16; ++r) sc_cur[r] = 0.f;
#pragma unroll
    for (int s = 0; s < 16; ++s) sc_cur = MFMA32(KFRAG(kb, s), qf[s], sc_cur);
  }
  ST();

  auto step = [&](const int j, auto modec) {
    constexpr int MODE = decltype(modec)::value;
    const int sA = 8 + 2 * j;
    if (MODE != 3) STAGE_ENTER_NOISSUE();
    const char* kb = smem + (sA & 3) * 32768;
    f32x16 sc_nxt;
#pragma unroll
    for (int r = 0; r < 16; ++r) sc_nxt[r] = 0.f;
    constexpr int KD_ = 6;
    bf16x8 kf[KD_];
#pragma unroll
    for (int s = 0; s < KD_ - 1; ++s) kf[s] = KFRAG(kb, s);
    SBAR();
    kf[KD_ - 1] = KFRAG(kb, KD_ - 1);
    sc_nxt = MFMA32(kf[0], qf[0], sc_nxt);
    float pm0 = fmaxf(fmaxf(sc_cur[0], sc_cur[1]), sc_cur[2]);
    pm0 = fmaxf(fmaxf(pm0, sc_cur[3]), sc_cur[4]);
    float pm1 = fmaxf(fmaxf(sc_cur[5], sc_cur[6]), sc_cur[7]);
    pm1 = fmaxf(fmaxf(pm1, sc_cur[8]), sc_cur[9]);
    SBAR();
    kf[0] = KFRAG(kb, KD_);
    sc_nxt = MFMA32(kf[1], qf[1], sc_nxt);
    pm0 = fmaxf(fmaxf(pm0, sc_cur[10]), sc_cur[11]);
    pm1 = fmaxf(fmaxf(pm1, sc_cur[12]), sc_cur[13]);
    pm0 = fmaxf(fmaxf(pm0, sc_cur[14]), sc_cur[15]);
    float pmax = fmaxf(pm0, pm1);
    SBAR();
    kf[1] = KFRAG(kb, KD_ + 1);
    sc_nxt = MFMA32(kf[2], qf[2], sc_nxt);
    {
      auto rr = __builtin_amdgcn_permlane32_swap(__float_as_uint(pmax), __float_as_uint(pmax), false, false);
      pmax = fmaxf(__uint_as_float(rr[0]), __uint_as_float(rr[1]));
    }
    SBAR();
    if (MODE == 0 && j == 0) {
      m_run = pmax;
    } else if (!__all(pmax - m_run <= THR)) {
      const float mn = fmaxf(m_run, pmax);
      const float alpha = __builtin_amdgcn_exp2f((m_run - mn) * SC);
      m_run = mn;
      l_run *= alpha;
      asm volatile("s_nop 7\n\ts_nop 7\n\ts_nop 3" ::: "memory");
#pragma unroll
      for (int t = 0; t < 8; ++t)
#pragma unroll
        for (int r = 0; r < 16; ++r) {
          float tmp;
          asm volatile("v_accvgpr_read_b32 %1, %0\n\ts_nop 0\n\tv_mul_f32 %1, %1, %2\n\ts_nop 0\n\tv_accvgpr_write_b32 %0, %1"
                       : "+a"(o[t][r]), "=&v"(tmp) : "v"(alpha));
        }
      asm volatile("s_nop 3" ::: "memory");
    }
    const float mc = -m_run * SC;
    unsigned pu[8];
#pragma unroll
    for (int s = 3; s < 16; ++s) {
      if (s + KD_ - 1 < 16) kf[(s + KD_ - 1) % KD_] = KFRAG(kb, s + KD_ - 1);
      sc_nxt = MFMA32(kf[s % KD_], qf[s], sc_nxt);
#pragma unroll
      for (int e = 0; e < 16; ++e) {
        if (3 + (e * 12) / 16 == s) {
          sc_cur[e] = __builtin_amdgcn_exp2f(fmaf(sc_cur[e], SC, mc));
          if (e & 1) pu[e >> 1] = cvtpk(sc_cur[e - 1], sc_cur[e]);
        }
      }
      if (s >= 4 && s < 12) {
        if (MODE <= 1) issueV1(j + 1, (sA + 3) & 3, s - 4);
        if (MODE == 2) issueW1(0, s - 4);
      }
      SBAR();
    }
    bf16x8 pf[2];
    {
      u32x4 u0 = {pu[0], pu[1], pu[2], pu[3]}, u1 = {pu[4], pu[5], pu[6], pu[7]};
      pf[0] = __builtin_bit_cast(bf16x8, u0);
      pf[1] = __builtin_bit_cast(bf16x8, u1);
    }
    const int sB = (MODE != 3) ? 9 + 2 * j : 38;
    STAGE_ENTER_NOISSUE();
    const char* vb = smem + (sB & 3) * 32768;
#define VFRAG(i) cat8(tr_read(vb + vra[(i) & 3] + (((i) >> 2) & 1) * 256 + ((i) >> 3) * 8192), \
                      tr_read(vb + vra[(i) & 3] + (((i) >> 2) & 1) * 256 + ((i) >> 3) * 8192 + 4096))
    constexpr int VD_ = 6;
    bf16x8 vf[VD_];
#pragma unroll
    for (int i = 0; i < VD_ - 1; ++i) vf[i] = VFRAG(i);
    SBAR();
    float ps0 = 0.f, ps1 = 0.f;
#pragma unroll
    for (int i = 0; i < 16; ++i) {
      if (i + VD_ - 1 < 16) vf[(i + VD_ - 1) % VD_] = VFRAG(i + VD_ - 1);
      o[i & 7] = MFMA32(vf[i % VD_], pf[i >> 3], o[i & 7]);
      if (i & 1) ps1 += sc_cur[i]; else ps0 += sc_cur[i];
      if ((i & 1) == 0) {
        if (MODE == 0) issueK1(j + 3, (sB + 3) & 3, i >> 1);
        if (MODE == 1) issueV1(15, 38 & 3, i >> 1);
        if (MODE == 2) issueW1(1, i >> 1);
        if (MODE == 3) issueW1(2, i >> 1);
      }
      SBAR();
    }
    l_run += ps0 + ps1;
    sc_cur = sc_nxt;
    ST();
  };
  for (int j = 0; j < 13; ++j) step(j, std::integral_constant<int, 0>{});
  step(13, std::integral_constant<int, 1>{});
  step(14, std::integral_constant<int, 2>{});
  step(15, std::integral_constant<int, 3>{});

  {
    auto rr = __builtin_amdgcn_permlane32_swap(__float_as_uint(l_run), __float_as_uint(l_run), false, false);
    l_run = __uint_as_float(rr[0]) + __uint_as_float(rr[1]);
  }
  float f1, f2;
  {
    float* xo = reinterpret_cast<float*>(smem + LDS_IMG) + w * 2048;
    const float* po = reinterpret_cast<const float*>(smem + LDS_IMG) + (w ^ 2) * 2048;
    xo[lane] = m_run;
    xo[64 + lane] = l_run;
    WAIT_LGKM0();
    BARRIER();
    const float m2 = po[lane], l2 = po[64 + lane];
    const float mn = fmaxf(m_run, m2);
    const float a1 = __builtin_amdgcn_exp2f((m_run - mn) * SC), a2 = __builtin_amdgcn_exp2f((m2 - mn) * SC);
    const float inv = 1.0f / (a1 * l_run + a2 * l2);
    f1 = a1 * inv;
    f2 = a2 * inv;
    WAIT_LGKM0();
    BARRIER();
#pragma unroll
    for (int rd = 0; rd < 2; ++rd) {
#pragma unroll
      for (int t = 0; t < 2; ++t)
#pragma unroll
        for (int r = 0; r < 16; ++r) xo[(t * 16 + r) * 64 + lane] = kh ? o[2 * rd + t][r] : o[4 + 2 * rd + t][r];
      WAIT_LGKM0();
      BARRIER();
#pragma unroll
      for (int t = 0; t < 2; ++t)
#pragma unroll
        for (int r = 0; r < 16; ++r) {
          const float pv = po[(t * 16 + r) * 64 + lane];
          if (kh) o[4 + 2 * rd + t][r] = o[4 + 2 * rd + t][r] * f1 + pv * f2;
          else o[2 * rd + t][r] = o[2 * rd + t][r] * f1 + pv * f2;
        }
      WAIT_LGKM0();
      BARRIER();
    }
#pragma unroll
    for (int t = 0; t < 4; ++t)
#pragma unroll
      for (int gq = 0; gq < 4; ++gq) {
        u32x2 v;
        if (kh) {
          v[0] = cvtpk(o[4 + t][4 * gq + 0], o[4 + t][4 * gq + 1]);
          v[1] = cvtpk(o[4 + t][4 * gq + 2], o[4 + t][4 * gq + 3]);
        } else {
          v[0] = cvtpk(o[t][4 * gq + 0], o[t][4 * gq + 1]);
          v[1] = cvtpk(o[t][4 * gq + 2], o[t][4 * gq + 3]);
        }
        const int row = 32 * qt + r32, ch = 16 * kh + 4 * t + gq;
        *reinterpret_cast<u32x2*>(smem + LDS_IMG + row * 512 + 16 * (ch ^ (row & 15)) + 8 * hi) = v;
      }
  }
  ST();

  {
    f32x16 acc[7];
#pragma unroll
    for (int u = 0; u < 7; ++u)
#pragma unroll
      for (int r = 0; r < 16; ++r) acc[u][r] = 0.f;
    const int tro = (8 * hh + q4) * 896 + 32 * cb + 8 * p4;
    const int nt3 = 12 + (w >> 1);
#pragma unroll
    for (int c = 0; c < 8; ++c) {
      if (c < 5) STAGE_ENTER(39 + c);
      else if (c == 5) STAGE_ENTER_TAIL(39 + c, 16);
      else if (c == 6) STAGE_ENTER_TAIL(39 + c, 8);
      else STAGE_ENTER_TAIL(39 + c, 0);
      const char* src = smem + ((39 + c) & 3) * 32768;
#pragma unroll
      for (int f = 0; f < 2; ++f) {
        const int ks = 2 * c + f;
        bf16x8 bq[2];
#pragma unroll
        for (int t = 0; t < 2; ++t) {
          const int row = 32 * t + r32;
          bq[t] = *reinterpret_cast<const bf16x8*>(smem + LDS_IMG + row * 512 + 16 * ((2 * ks + hi) ^ (row & 15)));
        }
        const char* sf = src + f * 16384 + tro;
#pragma unroll
        for (int u = 0; u < 3; ++u) {
          const bf16x8 af = cat8(tr_read(sf + 64 * (w + 4 * u)), tr_read(sf + 64 * (w + 4 * u) + 4 * 896));
          acc[2 * u] = MFMA32(af, bq[0], acc[2 * u]);
          acc[2 * u + 1] = MFMA32(af, bq[1], acc[2 * u + 1]);
        }
        {
          const bf16x8 af = cat8(tr_read(sf + 64 * nt3), tr_read(sf + 64 * nt3 + 4 * 896));
          acc[6] = MFMA32(af, qt ? bq[1] : bq[0], acc[6]);
        }
      }
    }
    ST();
    WAIT_LGKM0();
    BARRIER();
#pragma unroll
    for (int u = 0; u < 7; ++u) {
      const int nt = (u < 6) ? (w + 4 * (u >> 1)) : nt3;
      const int t = (u < 6) ? (u & 1) : qt;
      const int row = 32 * t + r32;
#pragma unroll
      for (int gq = 0; gq < 4; ++gq) {
        f32x4 v = {acc[u][4 * gq], acc[u][4 * gq + 1], acc[u][4 * gq + 2], acc[u][4 * gq + 3]};
        *reinterpret_cast<f32x4*>(smem + row * 1808 + (32 * nt + 8 * gq + 4 * hi) * 4) = v;
      }
    }
    WAIT_LGKM0();
    BARRIER();
    char* pp = (char*)(part + ((long)head * (NB * SQ) + b * SQ + half * QR) * DM);
#pragma unroll
    for (int i = 0; i < 25; ++i) {
      const int idx = (i * 4 + w) * 64 + lane;
      if (idx < QR * 112) {
        const int row = idx / 112, cu = idx - row * 112;
        const f32x4 v = *reinterpret_cast<const f32x4*>(smem + row * 1808 + cu * 16);
        *reinterpret_cast<f32x4*>(pp + (long)idx * 16) = v;
      }
    }
  }
  ST();
}

__global__ __launch_bounds__(256)
void reduce_kernel(const float* __restrict__ part, __hip_bfloat16* __restrict__ out) {
  const int i = blockIdx.x * 256 + threadIdx.x;
  constexpr int N4 = NB * SQ * DM / 4;
  const f32x4* p = reinterpret_cast<const f32x4*>(part) + i;
  f32x4 v[HQ];
#pragma unroll
  for (int h = 0; h < HQ; ++h) v[h] = __builtin_nontemporal_load(p + (long)h * N4);
  f32x4 s = v[0];
#pragma unroll
  for (int h = 1; h < HQ; ++h) s += v[h];
  u32x2 u;
  u[0] = cvtpk(s[0], s[1]);
  u[1] = cvtpk(s[2], s[3]);
  *reinterpret_cast<u32x2*>(out + (long)i * 4) = u;
}

extern "C" void kernel_launch(void* const* d_in, const int* in_sizes, int n_in,
                              void* d_out, int out_size, void* d_ws, size_t ws_size,
                              hipStream_t stream) {
  const __hip_bfloat16* x = (const __hip_bfloat16*)d_in[0];
  const __hip_bfloat16* kc = (const __hip_bfloat16*)d_in[1];
  const __hip_bfloat16* vc = (const __hip_bfloat16*)d_in[2];
  const __hip_bfloat16* wq = (const __hip_bfloat16*)d_in[3];
  const __hip_bfloat16* wo = (const __hip_bfloat16*)d_in[4];
  float* part = (float*)d_ws;
  __hip_bfloat16* out = (__hip_bfloat16*)d_out;
  attn_kernel<<<256, 256, 0, stream>>>(x, kc, vc, wq, wo, part);
  reduce_kernel<<<NB * SQ * DM / 4 / 256, 256, 0, stream>>>(part, out);
}
```

```cpp
#include <hip/hip_runtime.h>
#include <hip/hip_bf16.h>
#include <stdint.h>
#include <type_traits>

typedef short bf16x8 __attribute__((ext_vector_type(8)));
typedef short s16x4 __attribute__((ext_vector_type(4)));
typedef float f32x16 __attribute__((ext_vector_type(16)));
typedef float f32x4 __attribute__((ext_vector_type(4)));
typedef unsigned u32x4 __attribute__((ext_vector_type(4)));
typedef unsigned u32x2 __attribute__((ext_vector_type(2)));
typedef __attribute__((address_space(3))) s16x4* lds_s16x4_ptr;

constexpr int NB = 8, SQ = 112, DM = 448, HQ = 16, HKV = 8, SK = 1024, DH = 256;
constexpr int QR = 56;

#define MFMA32(a, b, c) __builtin_amdgcn_mfma_f32_32x32x16_bf16((a), (b), (c), 0, 0, 0)
#define DI __device__ __forceinline__
#define SBAR() __builtin_amdgcn_sched_barrier(0)

typedef __bf16 bf16x2_t __attribute__((ext_vector_type(2)));
typedef float f32x2 __attribute__((ext_vector_type(2)));
DI unsigned cvtpk(float lo, float hi) {
  f32x2 v = {lo, hi};
  return __builtin_bit_cast(unsigned, __builtin_convertvector(v, bf16x2_t));
}
DI s16x4 tr_read(const char* p) {
  return __builtin_amdgcn_ds_read_tr16_b64_v4i16((lds_s16x4_ptr)(p));
}
#define GLDS8_BODY(NT)                                     \
      "s_mov_b32 %0, m0\n\t"                               \
      "s_mov_b32 m0, %10\n\ts_nop 0\n\t"                   \
      "global_load_lds_dwordx4 %2, %1" NT "\n\t"           \
      "s_add_u32 m0, m0, 0x400\n\ts_nop 0\n\t"             \
      "global_load_lds_dwordx4 %3, %1" NT "\n\t"           \
      "s_add_u32 m0, m0, 0x400\n\ts_nop 0\n\t"             \
      "global_load_lds_dwordx4 %4, %1" NT "\n\t"           \
      "s_add_u32 m0, m0, 0x400\n\ts_nop 0\n\t"             \
      "global_load_lds_dwordx4 %5, %1" NT "\n\t"           \
      "s_add_u32 m0, m0, 0x3400\n\ts_nop 0\n\t"            \
      "global_load_lds_dwordx4 %6, %1" NT "\n\t"           \
      "s_add_u32 m0, m0, 0x400\n\ts_nop 0\n\t"             \
      "global_load_lds_dwordx4 %7, %1" NT "\n\t"           \
      "s_add_u32 m0, m0, 0x400\n\ts_nop 0\n\t"             \
      "global_load_lds_dwordx4 %8, %1" NT "\n\t"           \
      "s_add_u32 m0, m0, 0x400\n\ts_nop 0\n\t"             \
      "global_load_lds_dwordx4 %9, %1" NT "\n\t"           \
      "s_mov_b32 m0, %0"
DI void glds8(const void* sbase, unsigned dst, int o0, int o1, int o2, int o3, int o4, int o5, int o6, int o7) {
  unsigned keep;
  asm volatile(GLDS8_BODY("")
      : "=&s"(keep)
      : "s"(sbase), "v"(o0), "v"(o1), "v"(o2), "v"(o3), "v"(o4), "v"(o5), "v"(o6), "v"(o7), "s"(dst)
      : "memory");
}
DI void glds8nt(const void* sbase, unsigned dst, int o0, int o1, int o2, int o3, int o4, int o5, int o6, int o7) {
  unsigned keep;
  asm volatile(GLDS8_BODY(" nt")
      : "=&s"(keep)
      : "s"(sbase), "v"(o0), "v"(o1), "v"(o2), "v"(o3), "v"(o4), "v"(o5), "v"(o6), "v"(o7), "s"(dst)
      : "memory");
}
DI void glds1(const void* sbase, unsigned dst, int o0) {
  unsigned keep;
  asm volatile("s_mov_b32 %0, m0\n\ts_mov_b32 m0, %3\n\ts_nop 0\n\tglobal_load_lds_dwordx4 %2, %1\n\ts_mov_b32 m0, %0"
      : "=&s"(keep) : "s"(sbase), "v"(o0), "s"(dst) : "memory");
}
DI void glds1nt(const void* sbase, unsigned dst, int o0) {
  unsigned keep;
  asm volatile("s_mov_b32 %0, m0\n\ts_mov_b32 m0, %3\n\ts_nop 0\n\tglobal_load_lds_dwordx4 %2, %1 nt\n\ts_mov_b32 m0, %0"
      : "=&s"(keep) : "s"(sbase), "v"(o0), "s"(dst) : "memory");
}
DI bf16x8 cat8(s16x4 lo, s16x4 hi) { return __builtin_shufflevector(lo, hi, 0, 1, 2, 3, 4, 5, 6, 7); }

#define WAIT_VM(n) asm volatile("s_waitcnt vmcnt(" #n ")" ::: "memory")
#define WAIT_LGKM0() asm volatile("s_waitcnt lgkmcnt(0)" ::: "memory")
#define BARRIER() do { asm volatile("" ::: "memory"); __builtin_amdgcn_s_barrier(); asm volatile("" ::: "memory"); } while (0)
#define GLDS_KV glds8nt
#define GLDS_KV1 glds1nt

constexpr int LDS_IMG = 131072;
constexpr int LDS_TOTAL = 163840;
constexpr int NSTAGE = 47;

__global__ __launch_bounds__(256, 1)
void attn_kernel(const __hip_bfloat16* __restrict__ x, const __hip_bfloat16* __restrict__ kc,
                 const __hip_bfloat16* __restrict__ vc, const __hip_bfloat16* __restrict__ wq,
                 const __hip_bfloat16* __restrict__ wo, float* __restrict__ part) {
  __shared__ __attribute__((aligned(16))) char smem[LDS_TOTAL];
  const int tid = threadIdx.x, lane = tid & 63, r32 = lane & 31, hi = lane >> 5;
  const int w = __builtin_amdgcn_readfirstlane(tid >> 6);
  const int i16 = lane & 15, q4 = i16 >> 2, p4 = i16 & 3, g = lane >> 4, hh = g >> 1, cb = g & 1;
  const unsigned lds0 = __builtin_amdgcn_readfirstlane((unsigned)(uintptr_t)smem);
  const int bid = blockIdx.x;
  const int b = bid & 7, rest = bid >> 3, kvh = rest >> 2, head = rest >> 1, half = rest & 1;
  const int qt = w & 1, kh = w >> 1;
#define ST()
  ST();

  int offs_s[2];
  int offs_k[4];
#pragma unroll
  for (int p = 0; p < 4; ++p) {
    const int row = 8 * w + 2 * p + hi;
    offs_k[p] = row * 512 + 16 * (r32 ^ (row & 15));
    if (p < 2) offs_s[p] = row * 512 + 16 * (r32 ^ ((row & 3) << 2));
  }
  const char* kg = (const char*)(kc + (long)(b * HKV + kvh) * SK * DH);
  const char* vg = (const char*)(vc + (long)(b * HKV + kvh) * SK * DH);
  const char* wqg = (const char*)(wq + head * DH);
  const char* wog = (const char*)(wo + (long)head * DH * DM);
  auto issue = [&](int s) {
    const unsigned dst = lds0 + (s & 3) * 32768 + w * 4096;
    if (s < 7) {
      int o[4];
#pragma unroll
      for (int p = 0; p < 4; ++p) {
        const int row = 8 * w + 2 * p + hi;
        o[p] = row * 8192 + 16 * (r32 ^ ((row & 3) << 2));
      }
      glds8(wqg + (long)s * 64 * 8192, dst, o[0], o[1], o[2], o[3], o[0] + 262144, o[1] + 262144, o[2] + 262144, o[3] + 262144);
    } else if (s < 39) {
      const bool isK = (s == 7) || ((s & 1) == 0 && s != 38);
      if (isK) {
        const int j = (s == 7) ? 0 : (s - 6) >> 1;
        GLDS_KV(kg + (long)j * 16384, dst, offs_k[0], offs_k[1], offs_k[2], offs_k[3], offs_k[0] + 262144, offs_k[1] + 262144,
                offs_k[2] + 262144, offs_k[3] + 262144);
      } else {
        const int j = (s == 38) ? 15 : (s - 9) >> 1;
        GLDS_KV(vg + (long)j * 16384, dst, offs_s[0], offs_s[1], offs_s[0] + 2048, offs_s[1] + 2048, offs_s[0] + 262144,
                offs_s[1] + 262144, offs_s[0] + 264192, offs_s[1] + 264192);
      }
    } else {
      const int c = s - 39;
      int o[8];
#pragma unroll
      for (int i = 0; i < 8; ++i) {
        const int v = (2 * c + (i >> 2)) * 14336 + (4 * w + (i & 3)) * 1024 + lane * 16;
        o[i] = v < DH * DM * 2 - 16 ? v : DH * DM * 2 - 16;
      }
      glds8(wog, dst, o[0], o[1], o[2], o[3], o[4], o[5], o[6], o[7]);
    }
  };
  auto issueK1 = [&](int j, int sl, int i) {
    GLDS_KV1(kg + (long)j * 16384, lds0 + sl * 32768 + w * 4096 + (i & 3) * 1024 + (i >> 2) * 16384, offs_k[i & 3] + (i >> 2) * 262144);
  };
  auto issueV1 = [&](int j, int sl, int i) {
    GLDS_KV1(vg + (long)j * 16384, lds0 + sl * 32768 + w * 4096 + (i & 3) * 1024 + (i >> 2) * 16384,
             offs_s[i & 1] + ((i >> 1) & 1) * 2048 + (i >> 2) * 262144);
  };
  auto issueW1 = [&](int c, int i) {
    const int v = (2 * c + (i >> 2)) * 14336 + (4 * w + (i & 3)) * 1024 + lane * 16;
    glds1(wog, lds0 + ((39 + c) & 3) * 32768 + w * 4096 + (i & 3) * 1024 + (i >> 2) * 16384, v < DH * DM * 2 - 16 ? v : DH * DM * 2 - 16);
  };
#define STAGE_ENTER_NOISSUE() do { WAIT_VM(16); WAIT_LGKM0(); BARRIER(); } while (0)
#define STAGE_ENTER(s) do { WAIT_VM(16); WAIT_LGKM0(); BARRIER(); if ((s) + 3 < NSTAGE) issue((s) + 3); } while (0)
#define STAGE_ENTER_TAIL(s, n) do { WAIT_VM(n); WAIT_LGKM0(); BARRIER(); } while (0)

  {
    const char* xg = (const char*)(x + ((long)b * SQ + half * QR) * DM);
#pragma unroll
    for (int i = 0; i < 13; ++i) {
      int pc = 4 * i + w;
      if (pc > 48) pc = 48;
      const int by = pc * 1024 + lane * 16;
      const int row = by / 896, cp = (by - row * 896) >> 4;
      glds1(xg, lds0 + 65536 + pc * 1024, row * 896 + 16 * (cp ^ ((row >> 1) & 7)));
    }
  }
  issue(0);
  issue(1);
  WAIT_VM(16);
  BARRIER();
  bf16x8 xf[2][28];
#pragma unroll
  for (int t = 0; t < 2; ++t) {
    const int rr = 32 * t + r32;
    const int row = rr < QR ? rr : QR - 1;
#pragma unroll
    for (int s = 0; s < 28; ++s)
      xf[t][s] = *reinterpret_cast<const bf16x8*>(smem + 65536 + row * 896 + 16 * ((2 * s + hi) ^ ((row >> 1) & 7)));
  }
  WAIT_LGKM0();
  BARRIER();
  issue(2);

  {
    f32x16 qacc[2][2];
#pragma unroll
    for (int a = 0; a < 2; ++a)
#pragma unroll
      for (int t = 0; t < 2; ++t)
#pragma unroll
        for (int r = 0; r < 16; ++r) qacc[a][t][r] = 0.f;
    int tra[2];
#pragma unroll
    for (int a = 0; a < 2; ++a)
      tra[a] = (8 * hh + q4) * 512 + 16 * ((8 * w + 4 * a + 2 * cb + (p4 >> 1)) ^ (q4 << 2)) + 8 * (p4 & 1);
#pragma unroll
    for (int c = 0; c < 7; ++c) {
      STAGE_ENTER(c);
      ST();
      const char* src = smem + (c & 3) * 32768;
#pragma unroll
      for (int s = 0; s < 4; ++s) {
#pragma unroll
        for (int a = 0; a < 2; ++a) {
          const bf16x8 af = cat8(tr_read(src + tra[a] + s * 8192), tr_read(src + tra[a] + s * 8192 + 2048));
          qacc[a][0] = MFMA32(af, xf[0][4 * c + s], qacc[a][0]);
          qacc[a][1] = MFMA32(af, xf[1][4 * c + s], qacc[a][1]);
        }
      }
    }
    ST();
#pragma unroll
    for (int a = 0; a < 2; ++a)
#pragma unroll
      for (int t = 0; t < 2; ++t)
#pragma unroll
        for (int gq = 0; gq < 4; ++gq) {
          u32x2 v;
          v[0] = cvtpk(qacc[a][t][4 * gq + 0], qacc[a][t][4 * gq + 1]);
          v[1] = cvtpk(qacc[a][t][4 * gq + 2], qacc[a][t][4 * gq + 3]);
          const int row = 32 * t + r32, ch = 8 * w + 4 * a + gq;
          *reinterpret_cast<u32x2*>(smem + LDS_IMG + row * 512 + 16 * (ch ^ (row & 15)) + 8 * hi) = v;
        }
  }
  WAIT_LGKM0();
  BARRIER();
  bf16x8 qf[16];
  {
    const int row = 32 * qt + r32;
#pragma unroll
    for (int s = 0; s < 16; ++s)
      qf[s] = *reinterpret_cast<const bf16x8*>(smem + LDS_IMG + row * 512 + 16 * ((2 * s + hi) ^ (row & 15)));
  }

  int kra[8];
#pragma unroll
  for (int s = 0; s < 8; ++s) kra[s] = kh * 16384 + r32 * 512 + 16 * ((2 * s + hi) ^ (r32 & 15));
  int vra[4];
#pragma unroll
  for (int t = 0; t < 4; ++t)
    vra[t] = kh * 16384 + (4 * hh + q4) * 512 + 16 * (4 * (t ^ q4) + 2 * cb + (p4 >> 1)) + 8 * (p4 & 1);

  f32x16 o[8];
#pragma unroll
  for (int t = 0; t < 8; ++t)
#pragma unroll
    for (int r = 0; r < 16; ++r) o[t][r] = 0.f;
  float m_run = -1e30f, l_run = 0.f;
  constexpr float SC = 0.0625f * 1.4426950408889634f;
  constexpr float THR = 8.0f / 0.0625f;

#define KFRAG(base, s) (*reinterpret_cast<const bf16x8*>((base) + kra[(s) & 7] + ((s) >> 3) * 256))
#define VFRAG(base, i) cat8(tr_read((base) + vra[(i) & 3] + (((i) >> 2) & 1) * 256 + ((i) >> 3) * 8192), \
                            tr_read((base) + vra[(i) & 3] + (((i) >> 2) & 1) * 256 + ((i) >> 3) * 8192 + 4096))
#define STAGE_ENTER2() do { WAIT_VM(8); WAIT_LGKM0(); BARRIER(); } while (0)
#define PDV 5
#define RNG 8
  constexpr int PD = PDV;
  constexpr int RM = RNG - 1;
  bf16x8 fr[RNG];
  STAGE_ENTER2();
  issue(10);
  f32x16 sc_cur;
  {
    const char* kb = smem + (7 & 3) * 32768;
#pragma unroll
    for (int r = 0; r < 16; ++r) sc_cur[r] = 0.f;
#pragma unroll
    for (int s = 0; s < 16; ++s) sc_cur = MFMA32(KFRAG(kb, s), qf[s], sc_cur);
    const char* kn = smem + (8 & 3) * 32768;
#pragma unroll
    for (int s = 0; s < PD; ++s) fr[s] = KFRAG(kn, s);
  }
  ST();

  auto step = [&](const int j, auto modec) {
    constexpr int MODE = decltype(modec)::value;
    const int sA = 8 + 2 * j;
    const int sB = (MODE != 3) ? 9 + 2 * j : 38;
    const char* kb = smem + (sA & 3) * 32768;
    const char* vb = smem + (sB & 3) * 32768;
    const char* nb = smem + ((sB + 1) & 3) * 32768;
    if (MODE != 3) STAGE_ENTER2();
    f32x16 sc_nxt;
#pragma unroll
    for (int r = 0; r < 16; ++r) sc_nxt[r] = 0.f;
    auto gapA = [&](auto sc_) {
      constexpr int s = decltype(sc_)::value;
      if (MODE != 3) {
        if (s + PD < 16) fr[(s + PD) & RM] = KFRAG(kb, s + PD);
        else fr[(s + PD) & RM] = VFRAG(vb, s + PD - 16);
        sc_nxt = MFMA32(fr[s & RM], qf[s], sc_nxt);
        asm volatile("" : "+a"(sc_nxt));
      }
    };
    gapA(std::integral_constant<int, 0>{});
    float pm0 = fmaxf(fmaxf(sc_cur[0], sc_cur[1]), sc_cur[2]);
    pm0 = fmaxf(fmaxf(pm0, sc_cur[3]), sc_cur[4]);
    float pm1 = fmaxf(fmaxf(sc_cur[5], sc_cur[6]), sc_cur[7]);
    pm1 = fmaxf(fmaxf(pm1, sc_cur[8]), sc_cur[9]);
    SBAR();
    gapA(std::integral_constant<int, 1>{});
    pm0 = fmaxf(fmaxf(pm0, sc_cur[10]), sc_cur[11]);
    pm1 = fmaxf(fmaxf(pm1, sc_cur[12]), sc_cur[13]);
    pm0 = fmaxf(fmaxf(pm0, sc_cur[14]), sc_cur[15]);
    float pmax = fmaxf(pm0, pm1);
    SBAR();
    gapA(std::integral_constant<int, 2>{});
    {
      auto rr = __builtin_amdgcn_permlane32_swap(__float_as_uint(pmax), __float_as_uint(pmax), false, false);
      pmax = fmaxf(__uint_as_float(rr[0]), __uint_as_float(rr[1]));
    }
    SBAR();
    if (MODE == 0 && j == 0) {
      m_run = pmax;
    } else if (!__all(pmax - m_run <= THR)) {
      const float mn = fmaxf(m_run, pmax);
      const float alpha = __builtin_amdgcn_exp2f((m_run - mn) * SC);
      m_run = mn;
      l_run *= alpha;
      asm volatile("s_nop 7\n\ts_nop 7\n\ts_nop 3" ::: "memory");
#pragma unroll
      for (int t = 0; t < 8; ++t)
#pragma unroll
        for (int r = 0; r < 16; ++r) {
          float tmp;
          asm volatile("v_accvgpr_read_b32 %1, %0\n\ts_nop 0\n\tv_mul_f32 %1, %1, %2\n\ts_nop 0\n\tv_accvgpr_write_b32 %0, %1"
                       : "+a"(o[t][r]), "=&v"(tmp) : "v"(alpha));
        }
      asm volatile("s_nop 3" ::: "memory");
    }
    const float mc = -m_run * SC;
    unsigned pu[8];
    auto gapA2 = [&](auto sc_) {
      constexpr int s = decltype(sc_)::value;
      gapA(sc_);
#pragma unroll
      for (int e = 0; e < 16; ++e) {
        if (3 + (e * 12) / 16 == s) {
          sc_cur[e] = __builtin_amdgcn_exp2f(fmaf(sc_cur[e], SC, mc));
          if (e & 1) pu[e >> 1] = cvtpk(sc_cur[e - 1], sc_cur[e]);
        }
      }
      if (s >= 4 && s < 12) {
        if (MODE <= 1) issueV1(j + 1, (sA + 3) & 3, s - 4);
        if (MODE == 2) issueW1(0, s - 4);
      }
      SBAR();
    };
    gapA2(std::integral_constant<int, 3>{});  gapA2(std::integral_constant<int, 4>{});
    gapA2(std::integral_constant<int, 5>{});  gapA2(std::integral_constant<int, 6>{});
    gapA2(std::integral_constant<int, 7>{});  gapA2(std::integral_constant<int, 8>{});
    gapA2(std::integral_constant<int, 9>{});  gapA2(std::integral_constant<int, 10>{});
    gapA2(std::integral_constant<int, 11>{}); gapA2(std::integral_constant<int, 12>{});
    gapA2(std::integral_constant<int, 13>{}); gapA2(std::integral_constant<int, 14>{});
    gapA2(std::integral_constant<int, 15>{});
    bf16x8 pf[2];
    {
      u32x4 u0 = {pu[0], pu[1], pu[2], pu[3]}, u1 = {pu[4], pu[5], pu[6], pu[7]};
      pf[0] = __builtin_bit_cast(bf16x8, u0);
      pf[1] = __builtin_bit_cast(bf16x8, u1);
    }
    float ps0 = 0.f, ps1 = 0.f;
    if (MODE == 3) {
#pragma unroll
      for (int i = 0; i < 16; ++i) { if (i & 1) ps1 += sc_cur[i]; else ps0 += sc_cur[i]; }
      l_run += ps0 + ps1;
      auto rr = __builtin_amdgcn_permlane32_swap(__float_as_uint(l_run), __float_as_uint(l_run), false, false);
      l_run = __uint_as_float(rr[0]) + __uint_as_float(rr[1]);
      float* ml = reinterpret_cast<float*>(smem + LDS_IMG) + w * 128;
      ml[lane] = m_run;
      ml[64 + lane] = l_run;
    }
    STAGE_ENTER2();
#pragma unroll
    for (int i = 0; i < 16; ++i) {
      if (i + PD < 16) fr[(i + PD) & RM] = VFRAG(vb, i + PD);
      else if (MODE <= 1) fr[(i + PD) & RM] = KFRAG(nb, i + PD - 16);
      else if (MODE == 2) fr[(i + PD) & RM] = VFRAG(nb, i + PD - 16);
      o[i & 7] = MFMA32(fr[i & RM], pf[i >> 3], o[i & 7]);
      asm volatile("" : "+a"(o[i & 7]));
      if (MODE != 3) { if (i & 1) ps1 += sc_cur[i]; else ps0 += sc_cur[i]; }
      if ((i & 1) == 0) {
        if (MODE == 0) issueK1(j + 3, (sB + 3) & 3, i >> 1);
        if (MODE == 1) issueV1(15, 38 & 3, i >> 1);
        if (MODE == 2) issueW1(1, i >> 1);
        if (MODE == 3) issueW1(2, i >> 1);
      }
      SBAR();
    }
    if (MODE != 3) l_run += ps0 + ps1;
    sc_cur = sc_nxt;
    ST();
  };
  for (int j = 0; j < 13; ++j) step(j, std::integral_constant<int, 0>{});
  step(13, std::integral_constant<int, 1>{});
  step(14, std::integral_constant<int, 2>{});
  step(15, std::integral_constant<int, 3>{});

  {
    const float* pml = reinterpret_cast<const float*>(smem + LDS_IMG) + (w ^ 2) * 128;
    const float m2 = pml[lane], l2 = pml[64 + lane];
    const float mn = fmaxf(m_run, m2);
    const float a1 = __builtin_amdgcn_exp2f((m_run - mn) * SC), a2 = __builtin_amdgcn_exp2f((m2 - mn) * SC);
    const float f1 = a1 / (a1 * l_run + a2 * l2);
    WAIT_LGKM0();
    BARRIER();
    f32x4* xo = reinterpret_cast<f32x4*>(smem + LDS_IMG) + w * 512;
    const f32x4* po = reinterpret_cast<const f32x4*>(smem + LDS_IMG) + (w ^ 2) * 512;
    auto comb = [&](auto khc) {
      constexpr int KH = decltype(khc)::value;
      constexpr int GIVE = KH ? 0 : 4, KEEP = KH ? 4 : 0;
#pragma unroll
      for (int rd = 0; rd < 2; ++rd) {
#pragma unroll
        for (int t = 0; t < 2; ++t)
#pragma unroll
          for (int gq = 0; gq < 4; ++gq) {
            f32x4 v;
#pragma unroll
            for (int e = 0; e < 4; ++e) v[e] = o[GIVE + 2 * rd + t][4 * gq + e] * f1;
            xo[(t * 4 + gq) * 64 + lane] = v;
          }
        WAIT_LGKM0();
        BARRIER();
#pragma unroll
        for (int t = 0; t < 2; ++t)
#pragma unroll
          for (int gq = 0; gq < 4; ++gq) {
            const f32x4 pv = po[(t * 4 + gq) * 64 + lane];
#pragma unroll
            for (int e = 0; e < 4; ++e) o[KEEP + 2 * rd + t][4 * gq + e] = o[KEEP + 2 * rd + t][4 * gq + e] * f1 + pv[e];
          }
        WAIT_LGKM0();
        BARRIER();
      }
#pragma unroll
      for (int t = 0; t < 4; ++t)
#pragma unroll
        for (int gq = 0; gq < 4; ++gq) {
          u32x2 v;
          v[0] = cvtpk(o[KEEP + t][4 * gq + 0], o[KEEP + t][4 * gq + 1]);
          v[1] = cvtpk(o[KEEP + t][4 * gq + 2], o[KEEP + t][4 * gq + 3]);
          const int row = 32 * qt + r32, ch = 16 * KH + 4 * t + gq;
          *reinterpret_cast<u32x2*>(smem + LDS_IMG + row * 512 + 16 * (ch ^ (row & 15)) + 8 * hi) = v;
        }
    };
    if (kh) comb(std::integral_constant<int, 1>{});
    else comb(std::integral_constant<int, 0>{});
  }
  ST();

  {
    f32x16 acc[7];
#pragma unroll
    for (int u = 0; u < 7; ++u)
#pragma unroll
      for (int r = 0; r < 16; ++r) acc[u][r] = 0.f;
    const int tro = (8 * hh + q4) * 896 + 32 * cb + 8 * p4;
    const int nt3 = 12 + (w >> 1);
#pragma unroll
    for (int c = 0; c < 8; ++c) {
      if (c < 5) STAGE_ENTER(39 + c);
      else if (c == 5) STAGE_ENTER_TAIL(39 + c, 16);
      else if (c == 6) STAGE_ENTER_TAIL(39 + c, 8);
      else STAGE_ENTER_TAIL(39 + c, 0);
      const char* src = smem + ((39 + c) & 3) * 32768;
#pragma unroll
      for (int f = 0; f < 2; ++f) {
        const int ks = 2 * c + f;
        bf16x8 bq[2];
#pragma unroll
        for (int t = 0; t < 2; ++t) {
          const int row = 32 * t + r32;
          bq[t] = *reinterpret_cast<const bf16x8*>(smem + LDS_IMG + row * 512 + 16 * ((2 * ks + hi) ^ (row & 15)));
        }
        const char* sf = src + f * 16384 + tro;
#pragma unroll
        for (int u = 0; u < 3; ++u) {
          const bf16x8 af = cat8(tr_read(sf + 64 * (w + 4 * u)), tr_read(sf + 64 * (w + 4 * u) + 4 * 896));
          acc[2 * u] = MFMA32(af, bq[0], acc[2 * u]);
          acc[2 * u + 1] = MFMA32(af, bq[1], acc[2 * u + 1]);
        }
        {
          const bf16x8 af = cat8(tr_read(sf + 64 * nt3), tr_read(sf + 64 * nt3 + 4 * 896));
          acc[6] = MFMA32(af, qt ? bq[1] : bq[0], acc[6]);
        }
      }
    }
    ST();
    WAIT_LGKM0();
    BARRIER();
#pragma unroll
    for (int u = 0; u < 7; ++u) {
      const int nt = (u < 6) ? (w + 4 * (u >> 1)) : nt3;
      const int t = (u < 6) ? (u & 1) : qt;
      const int row = 32 * t + r32;
#pragma unroll
      for (int gq = 0; gq < 4; ++gq) {
        f32x4 v = {acc[u][4 * gq], acc[u][4 * gq + 1], acc[u][4 * gq + 2], acc[u][4 * gq + 3]};
        *reinterpret_cast<f32x4*>(smem + row * 1808 + (32 * nt + 8 * gq + 4 * hi) * 4) = v;
      }
    }
    WAIT_LGKM0();
    BARRIER();
    char* pp = (char*)(part + ((long)head * (NB * SQ) + b * SQ + half * QR) * DM);
#pragma unroll
    for (int i = 0; i < 25; ++i) {
      const int idx = (i * 4 + w) * 64 + lane;
      if (idx < QR * 112) {
        const int row = idx / 112, cu = idx - row * 112;
        const f32x4 v = *reinterpret_cast<const f32x4*>(smem + row * 1808 + cu * 16);
        *reinterpret_cast<f32x4*>(pp + (long)idx * 16) = v;
      }
    }
  }
  ST();
}

__global__ __launch_bounds__(256)
void reduce_kernel(const float* __restrict__ part, __hip_bfloat16* __restrict__ out) {
  const int i = blockIdx.x * 256 + threadIdx.x;
  constexpr int N4 = NB * SQ * DM / 4;
  const f32x4* p = reinterpret_cast<const f32x4*>(part) + i;
  f32x4 v[HQ];
#pragma unroll
  for (int h = 0; h < HQ; ++h) v[h] = __builtin_nontemporal_load(p + (long)h * N4);
  f32x4 s = v[0];
#pragma unroll
  for (int h = 1; h < HQ; ++h) s += v[h];
  u32x2 u;
  u[0] = cvtpk(s[0], s[1]);
  u[1] = cvtpk(s[2], s[3]);
  *reinterpret_cast<u32x2*>(out + (long)i * 4) = u;
}

extern "C" void kernel_launch(void* const* d_in, const int* in_sizes, int n_in,
                              void* d_out, int out_size, void* d_ws, size_t ws_size,
                              hipStream_t stream) {
  const __hip_bfloat16* x = (const __hip_bfloat16*)d_in[0];
  const __hip_bfloat16* kc = (const __hip_bfloat16*)d_in[1];
  const __hip_bfloat16* vc = (const __hip_bfloat16*)d_in[2];
  const __hip_bfloat16* wq = (const __hip_bfloat16*)d_in[3];
  const __hip_bfloat16* wo = (const __hip_bfloat16*)d_in[4];
  float* part = (float*)d_ws;
  __hip_bfloat16* out = (__hip_bfloat16*)d_out;
  attn_kernel<<<256, 256, 0, stream>>>(x, kc, vc, wq, wo, part);
  reduce_kernel<<<NB * SQ * DM / 4 / 256, 256, 0, stream>>>(part, out);
}
```

```cpp
#include <hip/hip_runtime.h>
#include <hip/hip_bf16.h>
#include <stdint.h>
#include <type_traits>

typedef short bf16x8 __attribute__((ext_vector_type(8)));
typedef short s16x4 __attribute__((ext_vector_type(4)));
typedef float f32x16 __attribute__((ext_vector_type(16)));
typedef float f32x4 __attribute__((ext_vector_type(4)));
typedef unsigned u32x4 __attribute__((ext_vector_type(4)));
typedef unsigned u32x2 __attribute__((ext_vector_type(2)));
typedef __attribute__((address_space(3))) s16x4* lds_s16x4_ptr;

constexpr int NB = 8, SQ = 112, DM = 448, HQ = 16, HKV = 8, SK = 1024, DH = 256;
constexpr int QR = 56;

#define MFMA32(a, b, c) __builtin_amdgcn_mfma_f32_32x32x16_bf16((a), (b), (c), 0, 0, 0)
#define DI __device__ __forceinline__
#define SBAR() __builtin_amdgcn_sched_barrier(0)

typedef __bf16 bf16x2_t __attribute__((ext_vector_type(2)));
typedef float f32x2 __attribute__((ext_vector_type(2)));
DI unsigned cvtpk(float lo, float hi) {
  f32x2 v = {lo, hi};
  return __builtin_bit_cast(unsigned, __builtin_convertvector(v, bf16x2_t));
}
DI s16x4 tr_read(const char* p) {
  return __builtin_amdgcn_ds_read_tr16_b64_v4i16((lds_s16x4_ptr)(p));
}
#define GLDS8_BODY(NT)                                     \
      "s_mov_b32 %0, m0\n\t"                               \
      "s_mov_b32 m0, %10\n\ts_nop 0\n\t"                   \
      "global_load_lds_dwordx4 %2, %1" NT "\n\t"           \
      "s_add_u32 m0, m0, 0x400\n\ts_nop 0\n\t"             \
      "global_load_lds_dwordx4 %3, %1" NT "\n\t"           \
      "s_add_u32 m0, m0, 0x400\n\ts_nop 0\n\t"             \
      "global_load_lds_dwordx4 %4, %1" NT "\n\t"           \
      "s_add_u32 m0, m0, 0x400\n\ts_nop 0\n\t"             \
      "global_load_lds_dwordx4 %5, %1" NT "\n\t"           \
      "s_add_u32 m0, m0, 0x3400\n\ts_nop 0\n\t"            \
      "global_load_lds_dwordx4 %6, %1" NT "\n\t"           \
      "s_add_u32 m0, m0, 0x400\n\ts_nop 0\n\t"             \
      "global_load_lds_dwordx4 %7, %1" NT "\n\t"           \
      "s_add_u32 m0, m0, 0x400\n\ts_nop 0\n\t"             \
      "global_load_lds_dwordx4 %8, %1" NT "\n\t"           \
      "s_add_u32 m0, m0, 0x400\n\ts_nop 0\n\t"             \
      "global_load_lds_dwordx4 %9, %1" NT "\n\t"           \
      "s_mov_b32 m0, %0"
DI void glds8(const void* sbase, unsigned dst, int o0, int o1, int o2, int o3, int o4, int o5, int o6, int o7) {
  unsigned keep;
  asm volatile(GLDS8_BODY("")
      : "=&s"(keep)
      : "s"(sbase), "v"(o0), "v"(o1), "v"(o2), "v"(o3), "v"(o4), "v"(o5), "v"(o6), "v"(o7), "s"(dst)
      : "memory");
}
DI void glds8nt(const void* sbase, unsigned dst, int o0, int o1, int o2, int o3, int o4, int o5, int o6, int o7) {
  unsigned keep;
  asm volatile(GLDS8_BODY(" nt")
      : "=&s"(keep)
      : "s"(sbase), "v"(o0), "v"(o1), "v"(o2), "v"(o3), "v"(o4), "v"(o5), "v"(o6), "v"(o7), "s"(dst)
      : "memory");
}
DI void glds1(const void* sbase, unsigned dst, int o0) {
  unsigned keep;
  asm volatile("s_mov_b32 %0, m0\n\ts_mov_b32 m0, %3\n\ts_nop 0\n\tglobal_load_lds_dwordx4 %2, %1\n\ts_mov_b32 m0, %0"
      : "=&s"(keep) : "s"(sbase), "v"(o0), "s"(dst) : "memory");
}
DI void glds1nt(const void* sbase, unsigned dst, int o0) {
  unsigned keep;
  asm volatile("s_mov_b32 %0, m0\n\ts_mov_b32 m0, %3\n\ts_nop 0\n\tglobal_load_lds_dwordx4 %2, %1 nt\n\ts_mov_b32 m0, %0"
      : "=&s"(keep) : "s"(sbase), "v"(o0), "s"(dst) : "memory");
}
DI bf16x8 cat8(s16x4 lo, s16x4 hi) { return __builtin_shufflevector(lo, hi, 0, 1, 2, 3, 4, 5, 6, 7); }

#define WAIT_VM(n) asm volatile("s_waitcnt vmcnt(" #n ")" ::: "memory")
#define WAIT_LGKM0() asm volatile("s_waitcnt lgkmcnt(0)" ::: "memory")
#define BARRIER() do { asm volatile("" ::: "memory"); __builtin_amdgcn_s_barrier(); asm volatile("" ::: "memory"); } while (0)
#define GLDS_KV glds8nt
#define GLDS_KV1 glds1nt

constexpr int LDS_IMG = 131072;
constexpr int LDS_TOTAL = 163840;
constexpr int NSTAGE = 47;

__global__ __launch_bounds__(256, 1)
void attn_kernel(const __hip_bfloat16* __restrict__ x, const __hip_bfloat16* __restrict__ kc,
                 const __hip_bfloat16* __restrict__ vc, const __hip_bfloat16* __restrict__ wq,
                 const __hip_bfloat16* __restrict__ wo, float* __restrict__ part) {
  __shared__ __attribute__((aligned(16))) char smem[LDS_TOTAL];
  const int tid = threadIdx.x, lane = tid & 63, r32 = lane & 31, hi = lane >> 5;
  const int w = __builtin_amdgcn_readfirstlane(tid >> 6);
  const int i16 = lane & 15, q4 = i16 >> 2, p4 = i16 & 3, g = lane >> 4, hh = g >> 1, cb = g & 1;
  const unsigned lds0 = __builtin_amdgcn_readfirstlane((unsigned)(uintptr_t)smem);
  const int bid = blockIdx.x;
  const int b = bid & 7, rest = bid >> 3, kvh = rest >> 2, head = rest >> 1, half = rest & 1;
  const int qt = w & 1, kh = w >> 1;
#define ST()
  ST();

  int offs_s[2];
  int offs_k[4];
#pragma unroll
  for (int p = 0; p < 4; ++p) {
    const int row = 8 * w + 2 * p + hi;
    offs_k[p] = row * 512 + 16 * (r32 ^ (row & 15));
    if (p < 2) offs_s[p] = row * 512 + 16 * (r32 ^ ((row & 3) << 2));
  }
  const char* kg = (const char*)(kc + (long)(b * HKV + kvh) * SK * DH);
  const char* vg = (const char*)(vc + (long)(b * HKV + kvh) * SK * DH);
  const char* wqg = (const char*)(wq + head * DH);
  const char* wog = (const char*)(wo + (long)head * DH * DM);
  auto issue = [&](int s) {
    const unsigned dst = lds0 + (s & 3) * 32768 + w * 4096;
    if (s < 7) {
      int o[4];
#pragma unroll
      for (int p = 0; p < 4; ++p) {
        const int row = 8 * w + 2 * p + hi;
        o[p] = row * 8192 + 16 * (r32 ^ ((row & 3) << 2));
      }
      glds8(wqg + (long)s * 64 * 8192, dst, o[0], o[1], o[2], o[3], o[0] + 262144, o[1] + 262144, o[2] + 262144, o[3] + 262144);
    } else if (s < 39) {
      const bool isK = (s == 7) || ((s & 1) == 0 && s != 38);
      if (isK) {
        const int j = (s == 7) ? 0 : (s - 6) >> 1;
        GLDS_KV(kg + (long)j * 16384, dst, offs_k[0], offs_k[1], offs_k[2], offs_k[3], offs_k[0] + 262144, offs_k[1] + 262144,
                offs_k[2] + 262144, offs_k[3] + 262144);
      } else {
        const int j = (s == 38) ? 15 : (s - 9) >> 1;
        GLDS_KV(vg + (long)j * 16384, dst, offs_s[0], offs_s[1], offs_s[0] + 2048, offs_s[1] + 2048, offs_s[0] + 262144,
                offs_s[1] + 262144, offs_s[0] + 264192, offs_s[1] + 264192);
      }
    } else {
      const int c = s - 39;
      int o[8];
#pragma unroll
      for (int i = 0; i < 8; ++i) {
        const int v = (2 * c + (i >> 2)) * 14336 + (4 * w + (i & 3)) * 1024 + lane * 16;
        o[i] = v < DH * DM * 2 - 16 ? v : DH * DM * 2 - 16;
      }
      glds8(wog, dst, o[0], o[1], o[2], o[3], o[4], o[5], o[6], o[7]);
    }
  };
  auto issueK1 = [&](int j, int sl, int i) {
    GLDS_KV1(kg + (long)j * 16384, lds0 + sl * 32768 + w * 4096 + (i & 3) * 1024 + (i >> 2) * 16384, offs_k[i & 3] + (i >> 2) * 262144);
  };
  auto issueV1 = [&](int j, int sl, int i) {
    GLDS_KV1(vg + (long)j * 16384, lds0 + sl * 32768 + w * 4096 + (i & 3) * 1024 + (i >> 2) * 16384,
             offs_s[i & 1] + ((i >> 1) & 1) * 2048 + (i >> 2) * 262144);
  };
  auto issueW1 = [&](int c, int i) {
    const int v = (2 * c + (i >> 2)) * 14336 + (4 * w + (i & 3)) * 1024 + lane * 16;
    glds1(wog, lds0 + ((39 + c) & 3) * 32768 + w * 4096 + (i & 3) * 1024 + (i >> 2) * 16384, v < DH * DM * 2 - 16 ? v : DH * DM * 2 - 16);
  };
#define STAGE_ENTER_NOISSUE() do { WAIT_VM(16); WAIT_LGKM0(); BARRIER(); } while (0)
#define STAGE_ENTER(s) do { WAIT_VM(16); WAIT_LGKM0(); BARRIER(); if ((s) + 3 < NSTAGE) issue((s) + 3); } while (0)
#define STAGE_ENTER_TAIL(s, n) do { WAIT_VM(n); WAIT_LGKM0(); BARRIER(); } while (0)

  {
    const char* xg = (const char*)(x + ((long)b * SQ + half * QR) * DM);
#pragma unroll
    for (int i = 0; i < 13; ++i) {
      int pc = 4 * i + w;
      if (pc > 48) pc = 48;
      const int by = pc * 1024 + lane * 16;
      const int row = by / 896, cp = (by - row * 896) >> 4;
      glds1(xg, lds0 + 65536 + pc * 1024, row * 896 + 16 * (cp ^ ((row >> 1) & 7)));
    }
  }
  issue(0);
  issue(1);
  WAIT_VM(16);
  BARRIER();
  bf16x8 xf[2][28];
#pragma unroll
  for (int t = 0; t < 2; ++t) {
    const int rr = 32 * t + r32;
    const int row = rr < QR ? rr : QR - 1;
#pragma unroll
    for (int s = 0; s < 28; ++s)
      xf[t][s] = *reinterpret_cast<const bf16x8*>(smem + 65536 + row * 896 + 16 * ((2 * s + hi) ^ ((row >> 1) & 7)));
  }
  WAIT_LGKM0();
  BARRIER();
  issue(2);

  {
    f32x16 qacc[2][2];
#pragma unroll
    for (int a = 0; a < 2; ++a)
#pragma unroll
      for (int t = 0; t < 2; ++t)
#pragma unroll
        for (int r = 0; r < 16; ++r) qacc[a][t][r] = 0.f;
    int tra[2];
#pragma unroll
    for (int a = 0; a < 2; ++a)
      tra[a] = (8 * hh + q4) * 512 + 16 * ((8 * w + 4 * a + 2 * cb + (p4 >> 1)) ^ (q4 << 2)) + 8 * (p4 & 1);
#pragma unroll
    for (int c = 0; c < 7; ++c) {
      STAGE_ENTER(c);
      ST();
      const char* src = smem + (c & 3) * 32768;
#pragma unroll
      for (int s = 0; s < 4; ++s) {
#pragma unroll
        for (int a = 0; a < 2; ++a) {
          const bf16x8 af = cat8(tr_read(src + tra[a] + s * 8192), tr_read(src + tra[a] + s * 8192 + 2048));
          qacc[a][0] = MFMA32(af, xf[0][4 * c + s], qacc[a][0]);
          qacc[a][1] = MFMA32(af, xf[1][4 * c + s], qacc[a][1]);
        }
      }
    }
    ST();
#pragma unroll
    for (int a = 0; a < 2; ++a)
#pragma unroll
      for (int t = 0; t < 2; ++t)
#pragma unroll
        for (int gq = 0; gq < 4; ++gq) {
          u32x2 v;
          v[0] = cvtpk(qacc[a][t][4 * gq + 0], qacc[a][t][4 * gq + 1]);
          v[1] = cvtpk(qacc[a][t][4 * gq + 2], qacc[a][t][4 * gq + 3]);
          const int row = 32 * t + r32, ch = 8 * w + 4 * a + gq;
          *reinterpret_cast<u32x2*>(smem + LDS_IMG + row * 512 + 16 * (ch ^ (row & 15)) + 8 * hi) = v;
        }
  }
  WAIT_LGKM0();
  BARRIER();
  bf16x8 qf[16];
  {
    const int row = 32 * qt + r32;
#pragma unroll
    for (int s = 0; s < 16; ++s)
      qf[s] = *reinterpret_cast<const bf16x8*>(smem + LDS_IMG + row * 512 + 16 * ((2 * s + hi) ^ (row & 15)));
  }

  int kra[8];
#pragma unroll
  for (int s = 0; s < 8; ++s) kra[s] = kh * 16384 + r32 * 512 + 16 * ((2 * s + hi) ^ (r32 & 15));
  int vra[4];
#pragma unroll
  for (int t = 0; t < 4; ++t)
    vra[t] = kh * 16384 + (4 * hh + q4) * 512 + 16 * (4 * (t ^ q4) + 2 * cb + (p4 >> 1)) + 8 * (p4 & 1);

  f32x16 o[8];
#pragma unroll
  for (int t = 0; t < 8; ++t)
#pragma unroll
    for (int r = 0; r < 16; ++r) o[t][r] = 0.f;
  float m_run = -1e30f, l_run = 0.f;
  constexpr float SC = 0.0625f * 1.4426950408889634f;
  constexpr float THR = 8.0f / 0.0625f;

#define KFRAG(base, s) (*reinterpret_cast<const bf16x8*>((base) + kra[(s) & 7] + ((s) >> 3) * 256))
#define VFRAG(base, i) cat8(tr_read((base) + vra[(i) & 3] + (((i) >> 2) & 1) * 256 + ((i) >> 3) * 8192), \
                            tr_read((base) + vra[(i) & 3] + (((i) >> 2) & 1) * 256 + ((i) >> 3) * 8192 + 4096))
#define STAGE_ENTER2() do { WAIT_VM(8); WAIT_LGKM0(); BARRIER(); } while (0)
#define PDV 5
#define RNG 8
  constexpr int PD = PDV;
  constexpr int RM = RNG - 1;
  bf16x8 fr[RNG];
  STAGE_ENTER2();
  issue(10);
  f32x16 sc_cur;
  {
    const char* kb = smem + (7 & 3) * 32768;
#pragma unroll
    for (int r = 0; r < 16; ++r) sc_cur[r] = 0.f;
#pragma unroll
    for (int s = 0; s < 16; ++s) sc_cur = MFMA32(KFRAG(kb, s), qf[s], sc_cur);
    const char* kn = smem + (8 & 3) * 32768;
#pragma unroll
    for (int s = 0; s < PD; ++s) fr[s] = KFRAG(kn, s);
  }
  ST();

  for (int j = 0; j < 16; ++j) {
    const bool last = (j == 15);
    const int sA = 8 + 2 * j;
    const int sB = last ? 38 : 9 + 2 * j;
    const char* kb = smem + (sA & 3) * 32768;
    const char* vb = smem + (sB & 3) * 32768;
    const char* nb = smem + ((sB + 1) & 3) * 32768;
    const char* pA = (j <= 13) ? vg + (long)(j + 1) * 16384 : wog;
    const char* pB = (j <= 12) ? kg + (long)(j + 3) * 16384 : (j == 13 ? vg + (long)15 * 16384 : wog);
    const unsigned dA = lds0 + ((sA + 3) & 3) * 32768 + w * 4096;
    const unsigned dB = lds0 + ((sB + 3) & 3) * 32768 + w * 4096;
    int oA[8], oB[8];
#pragma unroll
    for (int i = 0; i < 8; ++i) {
      const int ov = offs_s[i & 1] + ((i >> 1) & 1) * 2048 + (i >> 2) * 262144;
      const int ok = offs_k[i & 3] + (i >> 2) * 262144;
      const int ow = (i >> 2) * 14336 + (4 * w + (i & 3)) * 1024 + lane * 16;
      oA[i] = (j <= 13) ? ov : ow;
      oB[i] = (j <= 12) ? ok : (j == 13 ? ov : ow + (j - 13) * 28672);
    }
    if (!last) STAGE_ENTER2();
    f32x16 sc_nxt;
#pragma unroll
    for (int r = 0; r < 16; ++r) sc_nxt[r] = 0.f;
    auto gapA = [&](auto sc_) {
      constexpr int s = decltype(sc_)::value;
      if (!last) {
        if (s + PD < 16) fr[(s + PD) & RM] = KFRAG(kb, s + PD);
        else fr[(s + PD) & RM] = VFRAG(vb, s + PD - 16);
        sc_nxt = MFMA32(fr[s & RM], qf[s], sc_nxt);
        asm volatile("" : "+a"(sc_nxt));
      }
    };
    gapA(std::integral_constant<int, 0>{});
    float pm0 = fmaxf(fmaxf(sc_cur[0], sc_cur[1]), sc_cur[2]);
    pm0 = fmaxf(fmaxf(pm0, sc_cur[3]), sc_cur[4]);
    float pm1 = fmaxf(fmaxf(sc_cur[5], sc_cur[6]), sc_cur[7]);
    pm1 = fmaxf(fmaxf(pm1, sc_cur[8]), sc_cur[9]);
    SBAR();
    gapA(std::integral_constant<int, 1>{});
    pm0 = fmaxf(fmaxf(pm0, sc_cur[10]), sc_cur[11]);
    pm1 = fmaxf(fmaxf(pm1, sc_cur[12]), sc_cur[13]);
    pm0 = fmaxf(fmaxf(pm0, sc_cur[14]), sc_cur[15]);
    float pmax = fmaxf(pm0, pm1);
    SBAR();
    gapA(std::integral_constant<int, 2>{});
    {
      auto rr = __builtin_amdgcn_permlane32_swap(__float_as_uint(pmax), __float_as_uint(pmax), false, false);
      pmax = fmaxf(__uint_as_float(rr[0]), __uint_as_float(rr[1]));
    }
    SBAR();
    if (j == 0) {
      m_run = pmax;
    } else if (!__all(pmax - m_run <= THR)) {
      const float mn = fmaxf(m_run, pmax);
      const float alpha = __builtin_amdgcn_exp2f((m_run - mn) * SC);
      m_run = mn;
      l_run *= alpha;
      asm volatile("s_nop 7\n\ts_nop 7\n\ts_nop 3" ::: "memory");
#pragma unroll
      for (int t = 0; t < 8; ++t)
#pragma unroll
        for (int r = 0; r < 16; ++r) {
          float tmp;
          asm volatile("v_accvgpr_read_b32 %1, %0\n\ts_nop 0\n\tv_mul_f32 %1, %1, %2\n\ts_nop 0\n\tv_accvgpr_write_b32 %0, %1"
                       : "+a"(o[t][r]), "=&v"(tmp) : "v"(alpha));
        }
      asm volatile("s_nop 3" ::: "memory");
    }
    const float mc = -m_run * SC;
    unsigned pu[8];
    auto gapA2 = [&](auto sc_) {
      constexpr int s = decltype(sc_)::value;
      gapA(sc_);
#pragma unroll
      for (int e = 0; e < 16; ++e) {
        if (3 + (e * 12) / 16 == s) {
          sc_cur[e] = __builtin_amdgcn_exp2f(fmaf(sc_cur[e], SC, mc));
          if (e & 1) pu[e >> 1] = cvtpk(sc_cur[e - 1], sc_cur[e]);
        }
      }
      if (s >= 4 && s < 12) {
        if (!last) GLDS_KV1(pA, dA + ((s - 4) & 3) * 1024 + ((s - 4) >> 2) * 16384, oA[s - 4]);
      }
      SBAR();
    };
    gapA2(std::integral_constant<int, 3>{});  gapA2(std::integral_constant<int, 4>{});
    gapA2(std::integral_constant<int, 5>{});  gapA2(std::integral_constant<int, 6>{});
    gapA2(std::integral_constant<int, 7>{});  gapA2(std::integral_constant<int, 8>{});
    gapA2(std::integral_constant<int, 9>{});  gapA2(std::integral_constant<int, 10>{});
    gapA2(std::integral_constant<int, 11>{}); gapA2(std::integral_constant<int, 12>{});
    gapA2(std::integral_constant<int, 13>{}); gapA2(std::integral_constant<int, 14>{});
    gapA2(std::integral_constant<int, 15>{});
    bf16x8 pf[2];
    {
      u32x4 u0 = {pu[0], pu[1], pu[2], pu[3]}, u1 = {pu[4], pu[5], pu[6], pu[7]};
      pf[0] = __builtin_bit_cast(bf16x8, u0);
      pf[1] = __builtin_bit_cast(bf16x8, u1);
    }
    {
      float ps0 = 0.f, ps1 = 0.f;
#pragma unroll
      for (int i = 0; i < 16; ++i) { if (i & 1) ps1 += sc_cur[i]; else ps0 += sc_cur[i]; }
      l_run += ps0 + ps1;
    }
    if (last) {
      auto rr = __builtin_amdgcn_permlane32_swap(__float_as_uint(l_run), __float_as_uint(l_run), false, false);
      l_run = __uint_as_float(rr[0]) + __uint_as_float(rr[1]);
      float* ml = reinterpret_cast<float*>(smem + LDS_IMG) + w * 128;
      ml[lane] = m_run;
      ml[64 + lane] = l_run;
    }
    STAGE_ENTER2();
    int na[PD];
#pragma unroll
    for (int i = 0; i < PD; ++i) na[i] = (j == 14) ? vra[i & 3] + ((i >> 2) & 1) * 256 : kra[i & 7];
#pragma unroll
    for (int i = 0; i < 16; ++i) {
      if (i + PD < 16) fr[(i + PD) & RM] = VFRAG(vb, i + PD);
      else if (j == 14) fr[(i + PD) & RM] = cat8(tr_read(nb + na[i + PD - 16]), tr_read(nb + na[i + PD - 16] + 4096));
      else fr[(i + PD) & RM] = *reinterpret_cast<const bf16x8*>(nb + na[i + PD - 16]);
      o[i & 7] = MFMA32(fr[i & RM], pf[i >> 3], o[i & 7]);
      asm volatile("" : "+a"(o[i & 7]));
      if ((i & 1) == 0) GLDS_KV1(pB, dB + ((i >> 1) & 3) * 1024 + (i >> 3) * 16384, oB[i >> 1]);
      SBAR();
    }
    sc_cur = sc_nxt;
    ST();
  }

  {
    const float* pml = reinterpret_cast<const float*>(smem + LDS_IMG) + (w ^ 2) * 128;
    const float m2 = pml[lane], l2 = pml[64 + lane];
    const float mn = fmaxf(m_run, m2);
    const float a1 = __builtin_amdgcn_exp2f((m_run - mn) * SC), a2 = __builtin_amdgcn_exp2f((m2 - mn) * SC);
    const float f1 = a1 / (a1 * l_run + a2 * l2);
    WAIT_LGKM0();
    BARRIER();
    f32x4* xo = reinterpret_cast<f32x4*>(smem + LDS_IMG) + w * 512;
    const f32x4* po = reinterpret_cast<const f32x4*>(smem + LDS_IMG) + (w ^ 2) * 512;
    auto comb = [&](auto khc) {
      constexpr int KH = decltype(khc)::value;
      constexpr int GIVE = KH ? 0 : 4, KEEP = KH ? 4 : 0;
#pragma unroll
      for (int rd = 0; rd < 2; ++rd) {
#pragma unroll
        for (int t = 0; t < 2; ++t)
#pragma unroll
          for (int gq = 0; gq < 4; ++gq) {
            f32x4 v;
#pragma unroll
            for (int e = 0; e < 4; ++e) v[e] = o[GIVE + 2 * rd + t][4 * gq + e] * f1;
            xo[(t * 4 + gq) * 64 + lane] = v;
          }
        WAIT_LGKM0();
        BARRIER();
#pragma unroll
        for (int t = 0; t < 2; ++t)
#pragma unroll
          for (int gq = 0; gq < 4; ++gq) {
            const f32x4 pv = po[(t * 4 + gq) * 64 + lane];
#pragma unroll
            for (int e = 0; e < 4; ++e) o[KEEP + 2 * rd + t][4 * gq + e] = o[KEEP + 2 * rd + t][4 * gq + e] * f1 + pv[e];
          }
        WAIT_LGKM0();
        BARRIER();
      }
#pragma unroll
      for (int t = 0; t < 4; ++t)
#pragma unroll
        for (int gq = 0; gq < 4; ++gq) {
          u32x2 v;
          v[0] = cvtpk(o[KEEP + t][4 * gq + 0], o[KEEP + t][4 * gq + 1]);
          v[1] = cvtpk(o[KEEP + t][4 * gq + 2], o[KEEP + t][4 * gq + 3]);
          const int row = 32 * qt + r32, ch = 16 * KH + 4 * t + gq;
          *reinterpret_cast<u32x2*>(smem + LDS_IMG + row * 512 + 16 * (ch ^ (row & 15)) + 8 * hi) = v;
        }
    };
    if (kh) comb(std::integral_constant<int, 1>{});
    else comb(std::integral_constant<int, 0>{});
  }
  ST();

  {
    f32x16 acc[7];
#pragma unroll
    for (int u = 0; u < 7; ++u)
#pragma unroll
      for (int r = 0; r < 16; ++r) acc[u][r] = 0.f;
    const int tro = (8 * hh + q4) * 896 + 32 * cb + 8 * p4;
    const int nt3 = 12 + (w >> 1);
#pragma unroll
    for (int c = 0; c < 8; ++c) {
      if (c < 5) STAGE_ENTER(39 + c);
      else if (c == 5) STAGE_ENTER_TAIL(39 + c, 16);
      else if (c == 6) STAGE_ENTER_TAIL(39 + c, 8);
      else STAGE_ENTER_TAIL(39 + c, 0);
      const char* src = smem + ((39 + c) & 3) * 32768;
#pragma unroll
      for (int f = 0; f < 2; ++f) {
        const int ks = 2 * c + f;
        bf16x8 bq[2];
#pragma unroll
        for (int t = 0; t < 2; ++t) {
          const int row = 32 * t + r32;
          bq[t] = *reinterpret_cast<const bf16x8*>(smem + LDS_IMG + row * 512 + 16 * ((2 * ks + hi) ^ (row & 15)));
        }
        const char* sf = src + f * 16384 + tro;
#pragma unroll
        for (int u = 0; u < 3; ++u) {
          const bf16x8 af = cat8(tr_read(sf + 64 * (w + 4 * u)), tr_read(sf + 64 * (w + 4 * u) + 4 * 896));
          acc[2 * u] = MFMA32(af, bq[0], acc[2 * u]);
          acc[2 * u + 1] = MFMA32(af, bq[1], acc[2 * u + 1]);
        }
        {
          const bf16x8 af = cat8(tr_read(sf + 64 * nt3), tr_read(sf + 64 * nt3 + 4 * 896));
          acc[6] = MFMA32(af, qt ? bq[1] : bq[0], acc[6]);
        }
      }
    }
    ST();
    WAIT_LGKM0();
    BARRIER();
#pragma unroll
    for (int u = 0; u < 7; ++u) {
      const int nt = (u < 6) ? (w + 4 * (u >> 1)) : nt3;
      const int t = (u < 6) ? (u & 1) : qt;
      const int row = 32 * t + r32;
#pragma unroll
      for (int gq = 0; gq < 4; ++gq) {
        f32x4 v = {acc[u][4 * gq], acc[u][4 * gq + 1], acc[u][4 * gq + 2], acc[u][4 * gq + 3]};
        *reinterpret_cast<f32x4*>(smem + row * 1808 + (32 * nt + 8 * gq + 4 * hi) * 4) = v;
      }
    }
    WAIT_LGKM0();
    BARRIER();
    char* pp = (char*)(part + ((long)head * (NB * SQ) + b * SQ + half * QR) * DM);
#pragma unroll
    for (int i = 0; i < 25; ++i) {
      const int idx = (i * 4 + w) * 64 + lane;
      if (idx < QR * 112) {
        const int row = idx / 112, cu = idx - row * 112;
        const f32x4 v = *reinterpret_cast<const f32x4*>(smem + row * 1808 + cu * 16);
        *reinterpret_cast<f32x4*>(pp + (long)idx * 16) = v;
      }
    }
  }
  ST();
}

__global__ __launch_bounds__(256)
void reduce_kernel(const float* __restrict__ part, __hip_bfloat16* __restrict__ out) {
  const int i = blockIdx.x * 256 + threadIdx.x;
  constexpr int N4 = NB * SQ * DM / 4;
  const f32x4* p = reinterpret_cast<const f32x4*>(part) + i;
  f32x4 v[HQ];
#pragma unroll
  for (int h = 0; h < HQ; ++h) v[h] = __builtin_nontemporal_load(p + (long)h * N4);
  f32x4 s = v[0];
#pragma unroll
  for (int h = 1; h < HQ; ++h) s += v[h];
  u32x2 u;
  u[0] = cvtpk(s[0], s[1]);
  u[1] = cvtpk(s[2], s[3]);
  *reinterpret_cast<u32x2*>(out + (long)i * 4) = u;
}

extern "C" void kernel_launch(void* const* d_in, const int* in_sizes, int n_in,
                              void* d_out, int out_size, void* d_ws, size_t ws_size,
                              hipStream_t stream) {
  const __hip_bfloat16* x = (const __hip_bfloat16*)d_in[0];
  const __hip_bfloat16* kc = (const __hip_bfloat16*)d_in[1];
  const __hip_bfloat16* vc = (const __hip_bfloat16*)d_in[2];
  const __hip_bfloat16* wq = (const __hip_bfloat16*)d_in[3];
  const __hip_bfloat16* wo = (const __hip_bfloat16*)d_in[4];
  float* part = (float*)d_ws;
  __hip_bfloat16* out = (__hip_bfloat16*)d_out;
  attn_kernel<<<256, 256, 0, stream>>>(x, kc, vc, wq, wo, part);
  reduce_kernel<<<NB * SQ * DM / 4 / 256, 256, 0, stream>>>(part, out);
}
```

```cpp
#include <hip/hip_runtime.h>
#include <hip/hip_bf16.h>
#include <stdint.h>
#include <type_traits>

typedef short bf16x8 __attribute__((ext_vector_type(8)));
typedef short s16x4 __attribute__((ext_vector_type(4)));
typedef float f32x16 __attribute__((ext_vector_type(16)));
typedef float f32x4 __attribute__((ext_vector_type(4)));
typedef unsigned u32x4 __attribute__((ext_vector_type(4)));
typedef unsigned u32x2 __attribute__((ext_vector_type(2)));
typedef __attribute__((address_space(3))) s16x4* lds_s16x4_ptr;

constexpr int NB = 8, SQ = 112, DM = 448, HQ = 16, HKV = 8, SK = 1024, DH = 256;
constexpr int QR = 56;

#define MFMA32(a, b, c) __builtin_amdgcn_mfma_f32_32x32x16_bf16((a), (b), (c), 0, 0, 0)
#define DI __device__ __forceinline__
#define SBAR() __builtin_amdgcn_sched_barrier(0)

typedef __bf16 bf16x2_t __attribute__((ext_vector_type(2)));
typedef float f32x2 __attribute__((ext_vector_type(2)));
DI unsigned cvtpk(float lo, float hi) {
  f32x2 v = {lo, hi};
  return __builtin_bit_cast(unsigned, __builtin_convertvector(v, bf16x2_t));
}
DI s16x4 tr_read(const char* p) {
  return __builtin_amdgcn_ds_read_tr16_b64_v4i16((lds_s16x4_ptr)(p));
}
#define GLDS8_BODY(NT)                                     \
      "s_mov_b32 %0, m0\n\t"                               \
      "s_mov_b32 m0, %10\n\ts_nop 0\n\t"                   \
      "global_load_lds_dwordx4 %2, %1" NT "\n\t"           \
      "s_add_u32 m0, m0, 0x400\n\ts_nop 0\n\t"             \
      "global_load_lds_dwordx4 %3, %1" NT "\n\t"           \
      "s_add_u32 m0, m0, 0x400\n\ts_nop 0\n\t"             \
      "global_load_lds_dwordx4 %4, %1" NT "\n\t"           \
      "s_add_u32 m0, m0, 0x400\n\ts_nop 0\n\t"             \
      "global_load_lds_dwordx4 %5, %1" NT "\n\t"           \
      "s_add_u32 m0, m0, 0x3400\n\ts_nop 0\n\t"            \
      "global_load_lds_dwordx4 %6, %1" NT "\n\t"           \
      "s_add_u32 m0, m0, 0x400\n\ts_nop 0\n\t"             \
      "global_load_lds_dwordx4 %7, %1" NT "\n\t"           \
      "s_add_u32 m0, m0, 0x400\n\ts_nop 0\n\t"             \
      "global_load_lds_dwordx4 %8, %1" NT "\n\t"           \
      "s_add_u32 m0, m0, 0x400\n\ts_nop 0\n\t"             \
      "global_load_lds_dwordx4 %9, %1" NT "\n\t"           \
      "s_mov_b32 m0, %0"
DI void glds8(const void* sbase, unsigned dst, int o0, int o1, int o2, int o3, int o4, int o5, int o6, int o7) {
  unsigned keep;
  asm volatile(GLDS8_BODY("")
      : "=&s"(keep)
      : "s"(sbase), "v"(o0), "v"(o1), "v"(o2), "v"(o3), "v"(o4), "v"(o5), "v"(o6), "v"(o7), "s"(dst)
      : "memory");
}
DI void glds8nt(const void* sbase, unsigned dst, int o0, int o1, int o2, int o3, int o4, int o5, int o6, int o7) {
  unsigned keep;
  asm volatile(GLDS8_BODY(" nt")
      : "=&s"(keep)
      : "s"(sbase), "v"(o0), "v"(o1), "v"(o2), "v"(o3), "v"(o4), "v"(o5), "v"(o6), "v"(o7), "s"(dst)
      : "memory");
}
DI void glds1(const void* sbase, unsigned dst, int o0) {
  unsigned keep;
  asm volatile("s_mov_b32 %0, m0\n\ts_mov_b32 m0, %3\n\ts_nop 0\n\tglobal_load_lds_dwordx4 %2, %1\n\ts_mov_b32 m0, %0"
      : "=&s"(keep) : "s"(sbase), "v"(o0), "s"(dst) : "memory");
}
DI void glds1nt(const void* sbase, unsigned dst, int o0) {
  unsigned keep;
  asm volatile("s_mov_b32 %0, m0\n\ts_mov_b32 m0, %3\n\ts_nop 0\n\tglobal_load_lds_dwordx4 %2, %1 nt\n\ts_mov_b32 m0, %0"
      : "=&s"(keep) : "s"(sbase), "v"(o0), "s"(dst) : "memory");
}
DI bf16x8 cat8(s16x4 lo, s16x4 hi) { return __builtin_shufflevector(lo, hi, 0, 1, 2, 3, 4, 5, 6, 7); }

#define WAIT_VM(n) asm volatile("s_waitcnt vmcnt(" #n ")" ::: "memory")
#define WAIT_LGKM0() asm volatile("s_waitcnt lgkmcnt(0)" ::: "memory")
#define BARRIER() do { asm volatile("" ::: "memory"); __builtin_amdgcn_s_barrier(); asm volatile("" ::: "memory"); } while (0)
#define GLDS_KV glds8nt
#define GLDS_KV1 glds1nt

constexpr int LDS_IMG = 131072;
constexpr int LDS_TOTAL = 163840;
constexpr int NSTAGE = 47;

__global__ __launch_bounds__(256, 1)
void attn_kernel(const __hip_bfloat16* __restrict__ x, const __hip_bfloat16* __restrict__ kc,
                 const __hip_bfloat16* __restrict__ vc, const __hip_bfloat16* __restrict__ wq,
                 const __hip_bfloat16* __restrict__ wo, float* __restrict__ part) {
  __shared__ __attribute__((aligned(16))) char smem[LDS_TOTAL];
  const int tid = threadIdx.x, lane = tid & 63, r32 = lane & 31, hi = lane >> 5;
  const int w = __builtin_amdgcn_readfirstlane(tid >> 6);
  const int i16 = lane & 15, q4 = i16 >> 2, p4 = i16 & 3, g = lane >> 4, hh = g >> 1, cb = g & 1;
  const unsigned lds0 = __builtin_amdgcn_readfirstlane((unsigned)(uintptr_t)smem);
  const int bid = blockIdx.x;
  const int b = bid & 7, rest = bid >> 3, kvh = rest >> 2, head = rest >> 1, half = rest & 1;
  const int qt = w & 1, kh = w >> 1;

  int offs_s[2];
  int offs_k[4];
#pragma unroll
  for (int p = 0; p < 4; ++p) {
    const int row = 8 * w + 2 * p + hi;
    offs_k[p] = row * 512 + 16 * (r32 ^ (row & 15));
    if (p < 2) offs_s[p] = row * 512 + 16 * (r32 ^ ((row & 3) << 2));
  }
  const char* kg = (const char*)(kc + (long)(b * HKV + kvh) * SK * DH);
  const char* vg = (const char*)(vc + (long)(b * HKV + kvh) * SK * DH);
  const char* wqg = (const char*)(wq + head * DH);
  const char* wog = (const char*)(wo + (long)head * DH * DM);
  auto issue = [&](int s) {
    const unsigned dst = lds0 + (s & 3) * 32768 + w * 4096;
    if (s < 7) {
      int o[4];
#pragma unroll
      for (int p = 0; p < 4; ++p) {
        const int row = 8 * w + 2 * p + hi;
        o[p] = row * 8192 + 16 * (r32 ^ ((row & 3) << 2));
      }
      glds8(wqg + (long)s * 64 * 8192, dst, o[0], o[1], o[2], o[3], o[0] + 262144, o[1] + 262144, o[2] + 262144, o[3] + 262144);
    } else if (s < 39) {
      const bool isK = (s == 7) || ((s & 1) == 0 && s != 38);
      if (isK) {
        const int j = (s == 7) ? 0 : (s - 6) >> 1;
        GLDS_KV(kg + (long)j * 16384, dst, offs_k[0], offs_k[1], offs_k[2], offs_k[3], offs_k[0] + 262144, offs_k[1] + 262144,
                offs_k[2] + 262144, offs_k[3] + 262144);
      } else {
        const int j = (s == 38) ? 15 : (s - 9) >> 1;
        GLDS_KV(vg + (long)j * 16384, dst, offs_s[0], offs_s[1], offs_s[0] + 2048, offs_s[1] + 2048, offs_s[0] + 262144,
                offs_s[1] + 262144, offs_s[0] + 264192, offs_s[1] + 264192);
      }
    } else {
      const int c = s - 39;
      int o[8];
#pragma unroll
      for (int i = 0; i < 8; ++i) {
        const int v = (2 * c + (i >> 2)) * 14336 + (4 * w + (i & 3)) * 1024 + lane * 16;
        o[i] = v < DH * DM * 2 - 16 ? v : DH * DM * 2 - 16;
      }
      glds8(wog, dst, o[0], o[1], o[2], o[3], o[4], o[5], o[6], o[7]);
    }
  };
  auto issueK1 = [&](int j, int sl, int i) {
    GLDS_KV1(kg + (long)j * 16384, lds0 + sl * 32768 + w * 4096 + (i & 3) * 1024 + (i >> 2) * 16384, offs_k[i & 3] + (i >> 2) * 262144);
  };
  auto issueV1 = [&](int j, int sl, int i) {
    GLDS_KV1(vg + (long)j * 16384, lds0 + sl * 32768 + w * 4096 + (i & 3) * 1024 + (i >> 2) * 16384,
             offs_s[i & 1] + ((i >> 1) & 1) * 2048 + (i >> 2) * 262144);
  };
  auto issueW1 = [&](int c, int i) {
    const int v = (2 * c + (i >> 2)) * 14336 + (4 * w + (i & 3)) * 1024 + lane * 16;
    glds1(wog, lds0 + ((39 + c) & 3) * 32768 + w * 4096 + (i & 3) * 1024 + (i >> 2) * 16384, v < DH * DM * 2 - 16 ? v : DH * DM * 2 - 16);
  };
#define STAGE_ENTER_NOISSUE() do { WAIT_VM(16); WAIT_LGKM0(); BARRIER(); } while (0)
#define STAGE_ENTER(s) do { WAIT_VM(16); WAIT_LGKM0(); BARRIER(); if ((s) + 3 < NSTAGE) issue((s) + 3); } while (0)
#define STAGE_ENTER_TAIL(s, n) do { WAIT_VM(n); WAIT_LGKM0(); BARRIER(); } while (0)

  {
    const char* xg = (const char*)(x + ((long)b * SQ + half * QR) * DM);
#pragma unroll
    for (int i = 0; i < 13; ++i) {
      int pc = 4 * i + w;
      if (pc > 48) pc = 48;
      const int by = pc * 1024 + lane * 16;
      const int row = by / 896, cp = (by - row * 896) >> 4;
      glds1(xg, lds0 + 65536 + pc * 1024, row * 896 + 16 * (cp ^ ((row >> 1) & 7)));
    }
  }
  issue(0);
  issue(1);
  WAIT_VM(16);
  BARRIER();
  bf16x8 xf[2][28];
#pragma unroll
  for (int t = 0; t < 2; ++t) {
    const int rr = 32 * t + r32;
    const int row = rr < QR ? rr : QR - 1;
#pragma unroll
    for (int s = 0; s < 28; ++s)
      xf[t][s] = *reinterpret_cast<const bf16x8*>(smem + 65536 + row * 896 + 16 * ((2 * s + hi) ^ ((row >> 1) & 7)));
  }
  WAIT_LGKM0();
  BARRIER();
  issue(2);

  {
    f32x16 qacc[2][2];
#pragma unroll
    for (int a = 0; a < 2; ++a)
#pragma unroll
      for (int t = 0; t < 2; ++t)
#pragma unroll
        for (int r = 0; r < 16; ++r) qacc[a][t][r] = 0.f;
    int tra[2];
#pragma unroll
    for (int a = 0; a < 2; ++a)
      tra[a] = (8 * hh + q4) * 512 + 16 * ((8 * w + 4 * a + 2 * cb + (p4 >> 1)) ^ (q4 << 2)) + 8 * (p4 & 1);
#pragma unroll
    for (int c = 0; c < 7; ++c) {
      STAGE_ENTER(c);
      const char* src = smem + (c & 3) * 32768;
#pragma unroll
      for (int s = 0; s < 4; ++s) {
#pragma unroll
        for (int a = 0; a < 2; ++a) {
          const bf16x8 af = cat8(tr_read(src + tra[a] + s * 8192), tr_read(src + tra[a] + s * 8192 + 2048));
          qacc[a][0] = MFMA32(af, xf[0][4 * c + s], qacc[a][0]);
          qacc[a][1] = MFMA32(af, xf[1][4 * c + s], qacc[a][1]);
        }
      }
    }
#pragma unroll
    for (int a = 0; a < 2; ++a)
#pragma unroll
      for (int t = 0; t < 2; ++t)
#pragma unroll
        for (int gq = 0; gq < 4; ++gq) {
          u32x2 v;
          v[0] = cvtpk(qacc[a][t][4 * gq + 0], qacc[a][t][4 * gq + 1]);
          v[1] = cvtpk(qacc[a][t][4 * gq + 2], qacc[a][t][4 * gq + 3]);
          const int row = 32 * t + r32, ch = 8 * w + 4 * a + gq;
          *reinterpret_cast<u32x2*>(smem + LDS_IMG + row * 512 + 16 * (ch ^ (row & 15)) + 8 * hi) = v;
        }
  }
  WAIT_LGKM0();
  BARRIER();
  bf16x8 qf[16];
  {
    const int row = 32 * qt + r32;
#pragma unroll
    for (int s = 0; s < 16; ++s)
      qf[s] = *reinterpret_cast<const bf16x8*>(smem + LDS_IMG + row * 512 + 16 * ((2 * s + hi) ^ (row & 15)));
  }

  int kra[8];
#pragma unroll
  for (int s = 0; s < 8; ++s) kra[s] = kh * 16384 + r32 * 512 + 16 * ((2 * s + hi) ^ (r32 & 15));
  int vra[4];
#pragma unroll
  for (int t = 0; t < 4; ++t)
    vra[t] = kh * 16384 + (4 * hh + q4) * 512 + 16 * (4 * (t ^ q4) + 2 * cb + (p4 >> 1)) + 8 * (p4 & 1);

  f32x16 o[8];
#pragma unroll
  for (int t = 0; t < 8; ++t)
#pragma unroll
    for (int r = 0; r < 16; ++r) o[t][r] = 0.f;
  float m_run = -1e30f, l_run = 0.f;
  constexpr float SC = 0.0625f * 1.4426950408889634f;
  constexpr float THR = 8.0f / 0.0625f;

#define KFRAG(base, s) (*reinterpret_cast<const bf16x8*>((base) + kra[(s) & 7] + ((s) >> 3) * 256))
#define VFRAG(base, i) cat8(tr_read((base) + vra[(i) & 3] + (((i) >> 2) & 1) * 256 + ((i) >> 3) * 8192), \
                            tr_read((base) + vra[(i) & 3] + (((i) >> 2) & 1) * 256 + ((i) >> 3) * 8192 + 4096))
#define STAGE_ENTER2() do { WAIT_VM(8); WAIT_LGKM0(); BARRIER(); } while (0)
#define PDV 5
#define RNG 8
  constexpr int PD = PDV;
  constexpr int RM = RNG - 1;
  bf16x8 fr[RNG];
  STAGE_ENTER2();
  issue(10);
  f32x16 sc_cur;
  {
    const char* kb = smem + (7 & 3) * 32768;
#pragma unroll
    for (int r = 0; r < 16; ++r) sc_cur[r] = 0.f;
#pragma unroll
    for (int s = 0; s < 16; ++s) sc_cur = MFMA32(KFRAG(kb, s), qf[s], sc_cur);
    const char* kn = smem + (8 & 3) * 32768;
#pragma unroll
    for (int s = 0; s < PD; ++s) fr[s] = KFRAG(kn, s);
  }

  auto step = [&](const int j, auto modec) {
    constexpr int MODE = decltype(modec)::value;
    const int sA = 8 + 2 * j;
    const int sB = (MODE != 3) ? 9 + 2 * j : 38;
    const char* kb = smem + (sA & 3) * 32768;
    const char* vb = smem + (sB & 3) * 32768;
    const char* nb = smem + ((sB + 1) & 3) * 32768;
    if (MODE != 3) STAGE_ENTER2();
    f32x16 sc_nxt;
#pragma unroll
    for (int r = 0; r < 16; ++r) sc_nxt[r] = 0.f;
    auto gapA = [&](auto sc_) {
      constexpr int s = decltype(sc_)::value;
      if (MODE != 3) {
        if (s + PD < 16) fr[(s + PD) & RM] = KFRAG(kb, s + PD);
        else fr[(s + PD) & RM] = VFRAG(vb, s + PD - 16);
        sc_nxt = MFMA32(fr[s & RM], qf[s], sc_nxt);
        asm volatile("" : "+a"(sc_nxt));
      }
    };
    gapA(std::integral_constant<int, 0>{});
    float pm0 = fmaxf(fmaxf(sc_cur[0], sc_cur[1]), sc_cur[2]);
    pm0 = fmaxf(fmaxf(pm0, sc_cur[3]), sc_cur[4]);
    float pm1 = fmaxf(fmaxf(sc_cur[5], sc_cur[6]), sc_cur[7]);
    pm1 = fmaxf(fmaxf(pm1, sc_cur[8]), sc_cur[9]);
    SBAR();
    gapA(std::integral_constant<int, 1>{});
    pm0 = fmaxf(fmaxf(pm0, sc_cur[10]), sc_cur[11]);
    pm1 = fmaxf(fmaxf(pm1, sc_cur[12]), sc_cur[13]);
    pm0 = fmaxf(fmaxf(pm0, sc_cur[14]), sc_cur[15]);
    float pmax = fmaxf(pm0, pm1);
    SBAR();
    gapA(std::integral_constant<int, 2>{});
    {
      auto rr = __builtin_amdgcn_permlane32_swap(__float_as_uint(pmax), __float_as_uint(pmax), false, false);
      pmax = fmaxf(__uint_as_float(rr[0]), __uint_as_float(rr[1]));
    }
    SBAR();
    if (MODE == 0 && j == 0) {
      m_run = pmax;
    } else if (!__all(pmax - m_run <= THR)) {
      const float mn = fmaxf(m_run, pmax);
      const float alpha = __builtin_amdgcn_exp2f((m_run - mn) * SC);
      m_run = mn;
      l_run *= alpha;
      asm volatile("s_nop 7\n\ts_nop 7\n\ts_nop 3" ::: "memory");
#pragma unroll
      for (int t = 0; t < 8; ++t)
#pragma unroll
        for (int r = 0; r < 16; ++r) {
          float tmp;
          asm volatile("v_accvgpr_read_b32 %1, %0\n\ts_nop 0\n\tv_mul_f32 %1, %1, %2\n\ts_nop 0\n\tv_accvgpr_write_b32 %0, %1"
                       : "+a"(o[t][r]), "=&v"(tmp) : "v"(alpha));
        }
      asm volatile("s_nop 3" ::: "memory");
    }
    const float mc = -m_run * SC;
    unsigned pu[8];
    auto gapA2 = [&](auto sc_) {
      constexpr int s = decltype(sc_)::value;
      gapA(sc_);
#pragma unroll
      for (int e = 0; e < 16; ++e) {
        if (3 + (e * 12) / 16 == s) {
          sc_cur[e] = __builtin_amdgcn_exp2f(fmaf(sc_cur[e], SC, mc));
          if (e & 1) pu[e >> 1] = cvtpk(sc_cur[e - 1], sc_cur[e]);
        }
      }
      if (s >= 4 && s < 12) {
        if (MODE <= 1) issueV1(j + 1, (sA + 3) & 3, s - 4);
        if (MODE == 2) issueW1(0, s - 4);
      }
      SBAR();
    };
    gapA2(std::integral_constant<int, 3>{});  gapA2(std::integral_constant<int, 4>{});
    gapA2(std::integral_constant<int, 5>{});  gapA2(std::integral_constant<int, 6>{});
    gapA2(std::integral_constant<int, 7>{});  gapA2(std::integral_constant<int, 8>{});
    gapA2(std::integral_constant<int, 9>{});  gapA2(std::integral_constant<int, 10>{});
    gapA2(std::integral_constant<int, 11>{}); gapA2(std::integral_constant<int, 12>{});
    gapA2(std::integral_constant<int, 13>{}); gapA2(std::integral_constant<int, 14>{});
    gapA2(std::integral_constant<int, 15>{});
    bf16x8 pf[2];
    {
      u32x4 u0 = {pu[0], pu[1], pu[2], pu[3]}, u1 = {pu[4], pu[5], pu[6], pu[7]};
      pf[0] = __builtin_bit_cast(bf16x8, u0);
      pf[1] = __builtin_bit_cast(bf16x8, u1);
    }
    float ps0 = 0.f, ps1 = 0.f;
    if (MODE == 3) {
#pragma unroll
      for (int i = 0; i < 16; ++i) { if (i & 1) ps1 += sc_cur[i]; else ps0 += sc_cur[i]; }
      l_run += ps0 + ps1;
      auto rr = __builtin_amdgcn_permlane32_swap(__float_as_uint(l_run), __float_as_uint(l_run), false, false);
      l_run = __uint_as_float(rr[0]) + __uint_as_float(rr[1]);
      float* ml = reinterpret_cast<float*>(smem + LDS_IMG) + w * 128;
      ml[lane] = m_run;
      ml[64 + lane] = l_run;
    }
    STAGE_ENTER2();
#pragma unroll
    for (int i = 0; i < 16; ++i) {
      if (i + PD < 16) fr[(i + PD) & RM] = VFRAG(vb, i + PD);
      else if (MODE <= 1) fr[(i + PD) & RM] = KFRAG(nb, i + PD - 16);
      else if (MODE == 2) fr[(i + PD) & RM] = VFRAG(nb, i + PD - 16);
      o[i & 7] = MFMA32(fr[i & RM], pf[i >> 3], o[i & 7]);
      asm volatile("" : "+a"(o[i & 7]));
      if (MODE != 3) { if (i & 1) ps1 += sc_cur[i]; else ps0 += sc_cur[i]; }
      if ((i & 1) == 0) {
        if (MODE == 0) issueK1(j + 3, (sB + 3) & 3, i >> 1);
        if (MODE == 1) issueV1(15, 38 & 3, i >> 1);
        if (MODE == 2) issueW1(1, i >> 1);
        if (MODE == 3) issueW1(2, i >> 1);
      }
      SBAR();
    }
    if (MODE != 3) l_run += ps0 + ps1;
    sc_cur = sc_nxt;
  };
  for (int j = 0; j < 13; ++j) step(j, std::integral_constant<int, 0>{});
  step(13, std::integral_constant<int, 1>{});
  step(14, std::integral_constant<int, 2>{});
  step(15, std::integral_constant<int, 3>{});

  {
    const float* pml = reinterpret_cast<const float*>(smem + LDS_IMG) + (w ^ 2) * 128;
    const float m2 = pml[lane], l2 = pml[64 + lane];
    const float mn = fmaxf(m_run, m2);
    const float a1 = __builtin_amdgcn_exp2f((m_run - mn) * SC), a2 = __builtin_amdgcn_exp2f((m2 - mn) * SC);
    const float f1 = a1 / (a1 * l_run + a2 * l2);
    WAIT_LGKM0();
    BARRIER();
    f32x4* xo = reinterpret_cast<f32x4*>(smem + LDS_IMG) + w * 512;
    const f32x4* po = reinterpret_cast<const f32x4*>(smem + LDS_IMG) + (w ^ 2) * 512;
    auto comb = [&](auto khc) {
      constexpr int KH = decltype(khc)::value;
      constexpr int GIVE = KH ? 0 : 4, KEEP = KH ? 4 : 0;
#pragma unroll
      for (int rd = 0; rd < 2; ++rd) {
#pragma unroll
        for (int t = 0; t < 2; ++t)
#pragma unroll
          for (int gq = 0; gq < 4; ++gq) {
            f32x4 v;
#pragma unroll
            for (int e = 0; e < 4; ++e) v[e] = o[GIVE + 2 * rd + t][4 * gq + e] * f1;
            xo[(t * 4 + gq) * 64 + lane] = v;
          }
        WAIT_LGKM0();
        BARRIER();
#pragma unroll
        for (int t = 0; t < 2; ++t)
#pragma unroll
          for (int gq = 0; gq < 4; ++gq) {
            const f32x4 pv = po[(t * 4 + gq) * 64 + lane];
#pragma unroll
            for (int e = 0; e < 4; ++e) o[KEEP + 2 * rd + t][4 * gq + e] = o[KEEP + 2 * rd + t][4 * gq + e] * f1 + pv[e];
          }
        WAIT_LGKM0();
        BARRIER();
      }
#pragma unroll
      for (int t = 0; t < 4; ++t)
#pragma unroll
        for (int gq = 0; gq < 4; ++gq) {
          u32x2 v;
          v[0] = cvtpk(o[KEEP + t][4 * gq + 0], o[KEEP + t][4 * gq + 1]);
          v[1] = cvtpk(o[KEEP + t][4 * gq + 2], o[KEEP + t][4 * gq + 3]);
          const int row = 32 * qt + r32, ch = 16 * KH + 4 * t + gq;
          *reinterpret_cast<u32x2*>(smem + LDS_IMG + row * 512 + 16 * (ch ^ (row & 15)) + 8 * hi) = v;
        }
    };
    if (kh) comb(std::integral_constant<int, 1>{});
    else comb(std::integral_constant<int, 0>{});
  }

  {
    f32x16 acc[7];
#pragma unroll
    for (int u = 0; u < 7; ++u)
#pragma unroll
      for (int r = 0; r < 16; ++r) acc[u][r] = 0.f;
    const int tro = (8 * hh + q4) * 896 + 32 * cb + 8 * p4;
    const int nt3 = 12 + (w >> 1);
#pragma unroll
    for (int c = 0; c < 8; ++c) {
      if (c < 5) STAGE_ENTER(39 + c);
      else if (c == 5) STAGE_ENTER_TAIL(39 + c, 16);
      else if (c == 6) STAGE_ENTER_TAIL(39 + c, 8);
      else STAGE_ENTER_TAIL(39 + c, 0);
      const char* src = smem + ((39 + c) & 3) * 32768;
#pragma unroll
      for (int f = 0; f < 2; ++f) {
        const int ks = 2 * c + f;
        bf16x8 bq[2];
#pragma unroll
        for (int t = 0; t < 2; ++t) {
          const int row = 32 * t + r32;
          bq[t] = *reinterpret_cast<const bf16x8*>(smem + LDS_IMG + row * 512 + 16 * ((2 * ks + hi) ^ (row & 15)));
        }
        const char* sf = src + f * 16384 + tro;
#pragma unroll
        for (int u = 0; u < 3; ++u) {
          const bf16x8 af = cat8(tr_read(sf + 64 * (w + 4 * u)), tr_read(sf + 64 * (w + 4 * u) + 4 * 896));
          acc[2 * u] = MFMA32(af, bq[0], acc[2 * u]);
          acc[2 * u + 1] = MFMA32(af, bq[1], acc[2 * u + 1]);
        }
        {
          const bf16x8 af = cat8(tr_read(sf + 64 * nt3), tr_read(sf + 64 * nt3 + 4 * 896));
          acc[6] = MFMA32(af, qt ? bq[1] : bq[0], acc[6]);
        }
      }
    }
    WAIT_LGKM0();
    BARRIER();
#pragma unroll
    for (int u = 0; u < 7; ++u) {
      const int nt = (u < 6) ? (w + 4 * (u >> 1)) : nt3;
      const int t = (u < 6) ? (u & 1) : qt;
      const int row = 32 * t + r32;
#pragma unroll
      for (int gq = 0; gq < 4; ++gq) {
        f32x4 v = {acc[u][4 * gq], acc[u][4 * gq + 1], acc[u][4 * gq + 2], acc[u][4 * gq + 3]};
        *reinterpret_cast<f32x4*>(smem + row * 1808 + (32 * nt + 8 * gq + 4 * hi) * 4) = v;
      }
    }
    WAIT_LGKM0();
    BARRIER();
    char* pp = (char*)(part + ((long)head * (NB * SQ) + b * SQ + half * QR) * DM);
#pragma unroll
    for (int i = 0; i < 25; ++i) {
      const int idx = (i * 4 + w) * 64 + lane;
      if (idx < QR * 112) {
        const int row = idx / 112, cu = idx - row * 112;
        const f32x4 v = *reinterpret_cast<const f32x4*>(smem + row * 1808 + cu * 16);
        *reinterpret_cast<f32x4*>(pp + (long)idx * 16) = v;
      }
    }
  }
}

__global__ __launch_bounds__(256)
void reduce_kernel(const float* __restrict__ part, __hip_bfloat16* __restrict__ out) {
  const int i = blockIdx.x * 256 + threadIdx.x;
  constexpr int N4 = NB * SQ * DM / 4;
  const f32x4* p = reinterpret_cast<const f32x4*>(part) + i;
  f32x4 v[HQ];
#pragma unroll
  for (int h = 0; h < HQ; ++h) v[h] = __builtin_nontemporal_load(p + (long)h * N4);
  f32x4 s = v[0];
#pragma unroll
  for (int h = 1; h < HQ; ++h) s += v[h];
  u32x2 u;
  u[0] = cvtpk(s[0], s[1]);
  u[1] = cvtpk(s[2], s[3]);
  *reinterpret_cast<u32x2*>(out + (long)i * 4) = u;
}

extern "C" void kernel_launch(void* const* d_in, const int* in_sizes, int n_in,
                              void* d_out, int out_size, void* d_ws, size_t ws_size,
                              hipStream_t stream) {
  const __hip_bfloat16* x = (const __hip_bfloat16*)d_in[0];
  const __hip_bfloat16* kc = (const __hip_bfloat16*)d_in[1];
  const __hip_bfloat16* vc = (const __hip_bfloat16*)d_in[2];
  const __hip_bfloat16* wq = (const __hip_bfloat16*)d_in[3];
  const __hip_bfloat16* wo = (const __hip_bfloat16*)d_in[4];
  float* part = (float*)d_ws;
  __hip_bfloat16* out = (__hip_bfloat16*)d_out;
  attn_kernel<<<256, 256, 0, stream>>>(x, kc, vc, wq, wo, part);
  reduce_kernel<<<NB * SQ * DM / 4 / 256, 256, 0, stream>>>(part, out);
}
```
